```python
import torch

def _softmax(x, axis=-1):
    return torch.softmax(x, dim=axis)
def _gelu(x):
    return torch.nn.functional.gelu(x, approximate="tanh")
def _silu(x):
    return torch.nn.functional.silu(x)
def _sigmoid(x):
    return torch.sigmoid(x)
def _layernorm(x, axis=-1, eps=1e-5):
    m = x.mean(dim=axis, keepdim=True)
    v = x.var(dim=axis, keepdim=True, unbiased=False)
    return (x - m) / torch.sqrt(v + eps)
def _scatter_add(tgt, idx, src, axis=0):
    return tgt.clone().index_add_(axis, idx, src)
def _segment_sum(src, seg, n):
    out = torch.zeros((n,) + src.shape[1:], dtype=src.dtype)
    return out.index_add_(0, seg, src)
def _pad(x, pw):
    flat = tuple(v for pair in reversed(pw) for v in pair)
    return torch.nn.functional.pad(x, flat)

kernel_name = 'gated_mix__sliding_block__linear_attn_block__prompt'

def setup_inputs():

    _w = (lambda a: (lambda t: t.to(torch.bfloat16) if t.is_floating_point() else t)(torch.as_tensor(a)))
    def _A_setup():
        B, S, D = 1, 2048, 640
        return {
            "x": _w((lambda *s: torch.randn(*s, dtype=torch.bfloat16) * 0.05)(B, S, D)),
            "wqkv": _w((lambda *s: torch.randn(*s, dtype=torch.bfloat16) * 0.8)(D, 3 * D)),
            "wo": _w((lambda *s: torch.randn(*s, dtype=torch.bfloat16) * 0.05)(D, D)),
        }
    _A_in = _A_setup()

    def _B_setup():
        B, S, D = 1, 2048, 640
        return {
            "x": _w((lambda *s: torch.randn(*s, dtype=torch.bfloat16) * 0.05)(B, S, D)),
            "wqkv": _w((lambda *s: torch.randn(*s, dtype=torch.bfloat16) * 1.6)(D, 3 * D)),
            "wo": _w((lambda *s: torch.randn(*s, dtype=torch.bfloat16) * 0.4)(D, D)),
        }
    _B_in = _B_setup()

    return {
        "A_x": _A_in["x"],
        "A_wqkv": _A_in["wqkv"],
        "A_wo": _A_in["wo"],
        "B_x": _B_in["x"],
        "B_wqkv": _B_in["wqkv"],
        "B_wo": _B_in["wo"],
        "gate_w": _w((lambda *s: torch.randn(*s, dtype=torch.bfloat16) * 0.05)(640) * 16.0),
    }

def reference(A_x, A_wqkv, A_wo, B_x, B_wqkv, B_wo, gate_w):
    def _A_ref(x, wqkv, wo):
        B, S, D = x.shape
        H = 32; Dh = D // H; W = 256
        qkv = (lambda x, *d: x.permute(*d))((x @ wqkv).reshape(B, S, 3, H, Dh), 2, 0, 3, 1, 4)
        q, k, v = qkv[0], qkv[1], qkv[2]
        scores = torch.einsum("bhqd,bhkd->bhqk", q, k) * (Dh ** -0.5)
        idx = torch.arange(S)
        mask = (idx[:, None] - idx[None, :] >= 0) & (idx[:, None] - idx[None, :] < W)
        scores = torch.where(mask[None, None], scores, -1e9)
        p = _softmax(scores, axis=-1)
        o = torch.einsum("bhqk,bhkd->bhqd", p, v)
        return (lambda x, *d: x.permute(*d))(o, 0, 2, 1, 3).reshape(B, S, D) @ wo

    def _B_ref(x, wqkv, wo):
        B, S, D = x.shape
        H = 32; Dh = D // H
        qkv = (lambda x, *d: x.permute(*d))((x @ wqkv).reshape(B, S, 3, H, Dh), 2, 0, 3, 1, 4)
        q, k, v = qkv[0], qkv[1], qkv[2]
        phi = lambda t: torch.where(t > 0, t + 1, torch.exp(t)) + 1e-6
        pq, pk = phi(q), phi(k)
        kv = torch.einsum("bhsd,bhse->bhde", pk, v)
        z = torch.einsum("bhsd->bhd", pk)
        num = torch.einsum("bhqd,bhde->bhqe", pq, kv)
        den = torch.einsum("bhqd,bhd->bhq", pq, z)[..., None] + 1e-6
        o = num / den
        return (lambda x, *d: x.permute(*d))(o, 0, 2, 1, 3).reshape(B, S, D) @ wo

    x = A_x
    g = _sigmoid((x * gate_w).sum(-1, keepdims=True))
    a = _A_ref(x, A_wqkv, A_wo)
    b = _B_ref(x, B_wqkv, B_wo)
    return g * a + (1 - g) * b
```

```cpp
#include <hip/hip_runtime.h>
#include <hip/hip_bf16.h>
typedef __hip_bfloat16 bf16;
constexpr int S=2048, D=640, H=32, DH=20, W=256;
__device__ inline float b2f(bf16 v){return __bfloat162float(v);}
__device__ inline bf16 f2b(float v){return __float2bfloat16(v);}
__global__ void k_qkv(const bf16* x,const bf16* wa,const bf16* wb,bf16* qa,bf16* qb){
  int n=blockIdx.x*64+threadIdx.x; int s=blockIdx.y;
  const bf16* w = n<1920? wa: wb; int nn = n<1920? n: n-1920;
  float acc=0; for(int k=0;k<D;k++) acc+=b2f(x[s*D+k])*b2f(w[k*1920+nn]);
  (n<1920?qa:qb)[s*1920+nn]=f2b(acc);
}
__global__ void k_attn(const bf16* qa,bf16* oa){
  int idx=blockIdx.x*64+threadIdx.x; int s=idx%S, h=idx/S;
  float q[DH],o[DH]; for(int d=0;d<DH;d++){q[d]=b2f(qa[s*1920+h*DH+d]);o[d]=0;}
  float m=-1e30f; int lo=s-W+1; if(lo<0)lo=0;
  for(int j=lo;j<=s;j++){float sc=0;for(int d=0;d<DH;d++)sc+=q[d]*b2f(qa[j*1920+640+h*DH+d]); sc*=0.22360679775f; m=fmaxf(m,sc);}
  float l=0;
  for(int j=lo;j<=s;j++){float sc=0;for(int d=0;d<DH;d++)sc+=q[d]*b2f(qa[j*1920+640+h*DH+d]); sc*=0.22360679775f; float p=__expf(sc-m); l+=p; for(int d=0;d<DH;d++)o[d]+=p*b2f(qa[j*1920+1280+h*DH+d]);}
  for(int d=0;d<DH;d++) oa[s*D+h*DH+d]=f2b(o[d]/l);
}
__device__ inline float phi(float t){return (t>0? t+1.f: __expf(t))+1e-6f;}
__global__ void k_kv(const bf16* qb,float* kv){
  int h=blockIdx.x,t=threadIdx.x; if(t>=420)return;
  float acc=0;
  if(t<400){int d=t/20,e=t%20; for(int s=0;s<S;s++) acc+=phi(b2f(qb[s*1920+640+h*DH+d]))*b2f(qb[s*1920+1280+h*DH+e]);}
  else {int d=t-400; for(int s=0;s<S;s++) acc+=phi(b2f(qb[s*1920+640+h*DH+d]));}
  kv[h*420+t]=acc;
}
__global__ void k_ob(const bf16* qb,const float* kv,bf16* ob){
  int idx=blockIdx.x*64+threadIdx.x; int s=idx%S,h=idx/S;
  float pq[DH]; float den=1e-6f; for(int d=0;d<DH;d++){pq[d]=phi(b2f(qb[s*1920+h*DH+d])); den+=pq[d]*kv[h*420+400+d];}
  for(int e=0;e<DH;e++){float n=0;for(int d=0;d<DH;d++)n+=pq[d]*kv[h*420+d*20+e]; ob[s*D+h*DH+e]=f2b(n/den);}
}
__global__ void k_out(const bf16* x,const bf16* gw,const bf16* oa,const bf16* ob,const bf16* woa,const bf16* wob,bf16* out){
  int n=blockIdx.x*64+threadIdx.x; int s=blockIdx.y;
  float gs=0,a=0,b=0; for(int k=0;k<D;k++){gs+=b2f(x[s*D+k])*b2f(gw[k]); a+=b2f(oa[s*D+k])*b2f(woa[k*D+n]); b+=b2f(ob[s*D+k])*b2f(wob[k*D+n]);}
  float g=1.f/(1.f+__expf(-gs)); out[s*D+n]=f2b(g*a+(1.f-g)*b);
}
extern "C" void kernel_launch(void* const* d_in, const int* in_sizes, int n_in, void* d_out, int out_size, void* d_ws, size_t ws_size, hipStream_t stream){
  const bf16* x=(const bf16*)d_in[0]; const bf16* wa=(const bf16*)d_in[1]; const bf16* woa=(const bf16*)d_in[2];
  const bf16* wb=(const bf16*)d_in[4]; const bf16* wob=(const bf16*)d_in[5]; const bf16* gw=(const bf16*)d_in[6];
  char* ws=(char*)d_ws; bf16* qa=(bf16*)ws; bf16* qb=qa+S*1920; bf16* oa=qb+S*1920; bf16* ob=oa+S*D; float* kv=(float*)(ob+S*D);
  k_qkv<<<dim3(60,S),64,0,stream>>>(x,wa,wb,qa,qb);
  k_attn<<<S*H/64,64,0,stream>>>(qa,oa);
  k_kv<<<H,448,0,stream>>>(qb,kv);
  k_ob<<<S*H/64,64,0,stream>>>(qb,kv,ob);
  k_out<<<dim3(10,S),64,0,stream>>>(x,gw,oa,ob,woa,wob,(bf16*)d_out);
}
```

```cpp
#include <hip/hip_runtime.h>
#include <stdint.h>
typedef __bf16 bf8 __attribute__((ext_vector_type(8)));
typedef __bf16 bf2 __attribute__((ext_vector_type(2)));
typedef float f4 __attribute__((ext_vector_type(4)));
typedef float f2 __attribute__((ext_vector_type(2)));
typedef unsigned u4 __attribute__((ext_vector_type(4)));
typedef unsigned u2 __attribute__((ext_vector_type(2)));
typedef short s4 __attribute__((ext_vector_type(4)));
typedef unsigned long long u64;
#define AS3 __attribute__((address_space(3)))
typedef __amdgpu_buffer_rsrc_t rsrc_t;

constexpr int XR = 0, CR = 65536, WR = CR + 30720, GATE = 157696, MISC = 158976, KF = MISC + 64, DBGL = KF + 2048;
constexpr int SCR = CR + 5 * 10240, KVPITCH = 5184, LDS_BYTES = 163840;
constexpr int WS_F1 = 1024, WS_F2 = 2048, WS_F3B = 4096, WS_F3A = 6144, WS_DBG = 65536;
constexpr int WS_KV = 1 << 20, WS_KVP = 7 << 20, WS_OB = 8 << 20, WS_OA = 11 << 20, WS_BYTES = 16 << 20;
#define PUB 3
#define CHK 7
constexpr int SPIN_MAX = 200000;

template <int N> __device__ __forceinline__ void waitv() { asm volatile("s_waitcnt vmcnt(%0)" ::"i"(N) : "memory"); }
__device__ __forceinline__ void waitvn(int n) { asm volatile("s_waitcnt vmcnt(%0)" ::"i"(n) : "memory"); }
__device__ __forceinline__ void waitl() { asm volatile("" ::: "memory"); __builtin_amdgcn_s_waitcnt(0xC07F); asm volatile("" ::: "memory"); }
__device__ __forceinline__ void bar() { asm volatile("" ::: "memory"); __builtin_amdgcn_s_barrier(); asm volatile("" ::: "memory"); }
template <int NL, int SC1> __device__ __forceinline__ void dma(u4 rs, int lds, int voff, int soff) {
  if constexpr (NL == 64 && !SC1)
    asm volatile("s_mov_b32 m0, %0\n\ts_nop 0\n\tbuffer_load_dwordx4 %1, %2, %3 offen lds" ::"s"(lds), "v"(voff), "s"(rs), "s"(soff) : "memory");
  else if constexpr (NL == 64)
    asm volatile("s_mov_b32 m0, %0\n\ts_nop 0\n\tbuffer_load_dwordx4 %1, %2, %3 offen sc1 lds" ::"s"(lds), "v"(voff), "s"(rs), "s"(soff) : "memory");
  else if constexpr (NL >= 32)
    asm volatile("s_mov_b32 m0, %0\n\ts_mov_b32 exec_hi, %4\n\tbuffer_load_dwordx4 %1, %2, %3 offen lds\n\ts_mov_b32 exec_hi, -1" ::"s"(lds), "v"(voff), "s"(rs), "s"(soff), "i"((1 << (NL - 32)) - 1) : "memory");
  else
    asm volatile("s_mov_b32 m0, %0\n\ts_mov_b64 exec, %4\n\tbuffer_load_dwordx4 %1, %2, %3 offen lds\n\ts_mov_b64 exec, -1" ::"s"(lds), "v"(voff), "s"(rs), "s"(soff), "i"((1 << NL) - 1) : "memory");
}
__device__ __forceinline__ u4 mkrs(const void* p, unsigned bytes) {
  u64 a = (u64)p;
  return u4{(unsigned)a, (unsigned)(a >> 32) & 0xffffu, bytes, 0x00020000u};
}
__device__ __forceinline__ u4 bld128(u4 rs, int voff, int soff) {
  u4 d; asm volatile("buffer_load_dwordx4 %0, %1, %2, %3 offen sc1" : "=v"(d) : "v"(voff), "s"(rs), "s"(soff) : "memory"); return d;
}
template <int OFF> __device__ __forceinline__ u2 bld64(u4 rs, int voff, int soff) {
  u2 d; asm volatile("buffer_load_dwordx2 %0, %1, %2, %3 offen offset:%4 sc1" : "=v"(d) : "v"(voff), "s"(rs), "s"(soff), "i"(OFF) : "memory"); return d;
}
__device__ __forceinline__ void waitv_on(int n, u4& a) { asm volatile("s_waitcnt vmcnt(%1)" : "+v"(a) : "i"(n) : "memory"); }
__device__ __forceinline__ void waitv_on(int n, u2& a, u2& b) { asm volatile("s_waitcnt vmcnt(%2)" : "+v"(a), "+v"(b) : "i"(n) : "memory"); }
#define SB() __builtin_amdgcn_sched_barrier(0)
__device__ __forceinline__ u4 lr128(int a) { return *(const AS3 u4*)(size_t)a; }
__device__ __forceinline__ u2 lr64(int a) { return *(const AS3 u2*)(size_t)a; }
__device__ __forceinline__ void lw128(int a, u4 v) { *(AS3 u4*)(size_t)a = v; }
__device__ __forceinline__ void lw64(int a, u2 v) { *(AS3 u2*)(size_t)a = v; }
__device__ __forceinline__ u2 ltr(int a) {
  s4 v = __builtin_amdgcn_ds_read_tr16_b64_v4i16((AS3 s4*)(size_t)a);
  return __builtin_bit_cast(u2, v);
}
__device__ __forceinline__ u4 cat(u2 a, u2 b) { return u4{a.x, a.y, b.x, b.y}; }
__device__ __forceinline__ f4 mf(u4 a, u4 b, f4 c) {
  return __builtin_amdgcn_mfma_f32_16x16x32_bf16(__builtin_bit_cast(bf8, a), __builtin_bit_cast(bf8, b), c, 0, 0, 0);
}
__device__ __forceinline__ unsigned pk(float a, float b) {
  f2 v = {a, b};
  return __builtin_bit_cast(unsigned, __builtin_convertvector(v, bf2));
}
__device__ __forceinline__ u2 pk4(f4 v) { return u2{pk(v.x, v.y), pk(v.z, v.w)}; }
__device__ __forceinline__ float phi(float t) { return (t > 0.f ? t + 1.f : __builtin_amdgcn_exp2f(t * 1.44269504089f)) + 1e-6f; }
__device__ __forceinline__ f4 phi4(f4 v) { return f4{phi(v.x), phi(v.y), phi(v.z), phi(v.w)}; }
__device__ __forceinline__ u2 swap16(unsigned a, unsigned b) {
  auto r = __builtin_amdgcn_permlane16_swap(a, b, false, false);
  return u2{r[0], r[1]};
}
__device__ __forceinline__ u2 swap32(unsigned a, unsigned b) {
  auto r = __builtin_amdgcn_permlane32_swap(a, b, false, false);
  return u2{r[0], r[1]};
}
__device__ __forceinline__ float asf(unsigned x) { return __builtin_bit_cast(float, x); }
__device__ __forceinline__ unsigned asu(float x) { return __builtin_bit_cast(unsigned, x); }
__device__ __forceinline__ void flag_store(char* ws, int off, u64 tag) {
  __hip_atomic_store((u64*)(ws + off), tag, __ATOMIC_RELAXED, __HIP_MEMORY_SCOPE_AGENT);
}
__device__ __forceinline__ void poll(char* ws, int off, bool act, u64 tag) {
#pragma unroll 1
  for (int it = 0; it < SPIN_MAX; ++it) {
    u64 v = __hip_atomic_load((u64*)(ws + off), __ATOMIC_RELAXED, __HIP_MEMORY_SCOPE_AGENT);
    if (__builtin_amdgcn_ballot_w64(act && v != tag) == 0) break;
    __builtin_amdgcn_s_sleep(2);
  }
  asm volatile("" ::: "memory");
}
#define STAMP(i)

__global__ __launch_bounds__(512, 2) void fused_kernel(const void* __restrict__ px, const void* __restrict__ pwa,
                                                       const void* __restrict__ pwoa, const void* __restrict__ pwb,
                                                       const void* __restrict__ pwob, const void* __restrict__ pgw,
                                                       char* __restrict__ out, char* __restrict__ ws) {
  const int tid = threadIdx.x, l = tid & 63, w = __builtin_amdgcn_readfirstlane(tid >> 6);
  const int bid = blockIdx.x, xcd = bid & 7, idx = bid >> 3;
  {
    u64 pc; unsigned sz;
    asm volatile("s_getpc_b64 %0\n.Lfk_start:\n s_mov_b32 %1, .Lfk_end-.Lfk_start" : "=s"(pc), "=s"(sz));
    unsigned o = (unsigned)((((2 * idx + w) & 7) * 64 + l) * 128);
    if (w < 2 && o < sz)
      asm volatile("s_mov_b32 m0, %2\n\ts_nop 0\n\tglobal_load_lds_dword %0, %1" ::"v"(o), "s"(pc), "s"(DBGL + 256) : "memory");
  }
  const int r = l & 15, g = l >> 4, q = r >> 2, p = r & 3, mw = w & 3, nw = w >> 2;
  const int cq = xcd >> 1, tt = 8 * (xcd & 1) + (idx & 7), cs = idx >> 3;
  const bool isB = cs >= 2;
  const int pair = 4 * cq + 2 * (cs & 1) + ((cs < 2 ? 1 : 0) ^ (cq & 1));
  const int rot = (idx * 3 + (idx >> 3)) % 10;
  const u4 rX = mkrs(px, 2048 * 1280), rWA = mkrs(pwa, 640 * 3840), rWB = mkrs(pwb, 640 * 3840);
  const u4 rW1 = mkrs(isB ? pwb : pwa, 640 * 3840), rWSd = mkrs(ws, WS_BYTES);
  const rsrc_t rWS = __builtin_amdgcn_make_buffer_rsrc((void*)ws, 0, WS_BYTES, 0x00020000);
  STAMP(0);
  int xv0, xv1, wv[3];
  {
    int row = 16 * w + (l >> 3);
    xv0 = row * 1280 + (((l & 7) ^ ((row >> 1) & 7)) << 4);
    xv1 = (xv0 ^ 64) + 8 * 1280;
    const int base0 = isB ? 1280 + 160 * (2 * cq + cs - 2) : 1280 + 1280 * cs + 320 * cq;
    const int delta = isB ? 1120 : 0;
#pragma unroll
    for (int i = 0; i < 3; ++i) {
      int ci = 64 * i + l, rw = ci / 20, pos = ci - rw * 20;
      int t = pos ^ (2 * (w & 1));
      int c = t >= 10 ? t ^ 1 : t;
      wv[i] = (8 * w + rw) * 3840 + base0 + 16 * c + (c >= 10 ? delta : 0);
    }
  }
  const int xs0 = tt * (128 * 1280);
  auto kchunk = [&](int s) { int k = s + rot; return k >= 10 ? k - 10 : k; };
  auto issx = [&](int s, int i) { dma<64, 0>(rX, XR + (s & 3) * 16384 + w * 2048 + i * 1024, i ? xv1 : xv0, xs0 + kchunk(s) * 128); };
  auto issw = [&](int s, int i) {
    int wd = WR + ((s + 2) % 3) * 20480 + w * 2560 + i * 1024, so = kchunk(s) * 245760;
    if (i < 2) dma<64, 0>(rW1, wd, wv[i], so); else dma<32, 0>(rW1, wd, wv[2], so);
  };
#pragma unroll
  for (int s = 0; s < 3; ++s) { issx(s, 0); issx(s, 1); issw(s, 0); issw(s, 1); issw(s, 2); }
  SB();
  u64 tag;
  asm volatile("s_load_dwordx2 %0, %1, 0x0" : "=s"(tag) : "s"(ws) : "memory");
  int qva, qvb;
  {
    int pr = l / 5, c = l - pr * 5;
    int kb = 8 * w + (pr >> 1);
    qva = (kb + 4 * (pr & 1)) * 3840 + 80 * pair + 16 * c;
    qvb = (kb + 4 * ((pr & 1) ^ 1)) * 3840 + 80 * pair + 16 * c;
  }
  auto iss2 = [&](int rs, int i) {
    dma<40, 0>(i ? rWB : rWA, CR + (rs % 9) * 10240 + w * 640 + i * 5120, i ? qvb : qva, kchunk((rs + 6) % 10) * 245760);
  };
  auto issue2 = [&](int rs) { iss2(rs, 0); iss2(rs, 1); };
  int wa[5];
#pragma unroll
  for (int nt = 0; nt < 5; ++nt) {
    int byteA = 160 * nw + 32 * nt + 8 * p;
    int byteB = nt < 4 ? 160 * (nt >> 1) + 80 * nw + 40 * (nt & 1) + 8 * p : 160 * (p >> 1) + 80 * nw + 40 * (p & 1) + 32;
    int by = isB ? byteB : byteA;
    if (by >= 160) by ^= 16;
    by ^= (g & 1) << 5;
    wa[nt] = WR + (8 * g + q) * 320 + by;
  }
  const int xa0 = XR + (32 * mw + r) * 128 + ((g ^ (r >> 1)) << 4), xa1 = xa0 ^ 64;
  if (tid < 4) *(AS3 unsigned*)(size_t)(MISC + 4 * tid) = 0;

  u4 xk[10][2][2];
  u4 wf[2][5];
  f4 acc[2][5];
#pragma unroll
  for (int i = 0; i < 5; ++i) acc[0][i] = acc[1][i] = f4{0, 0, 0, 0};
  auto rdx = [&](int s, int kk) {
    int xo = (s & 3) * 16384;
    xk[s][kk][0] = lr128((kk ? xa1 : xa0) + xo);
    xk[s][kk][1] = lr128((kk ? xa1 : xa0) + xo + 2048);
  };
  auto rdw = [&](int s, int kk, int nt) {
    int wo = ((s + 2) % 3) * 20480 + kk * 10240;
    wf[kk][nt] = cat(ltr(wa[nt] + wo), ltr(wa[nt] + wo + 1280));
  };
  auto mm1 = [&](int s, int kk, int nt) {
    acc[0][nt] = mf(xk[s][kk][0], wf[kk][nt], acc[0][nt]);
    acc[1][nt] = mf(xk[s][kk][1], wf[kk][nt], acc[1][nt]);
  };
  int gv;
  {
    int ci = 10 * w + l, kc = (ci >> 3) + 6 + rot;
    kc = kc >= 20 ? kc - 20 : (kc >= 10 ? kc - 10 : kc);
    gv = kc * 128 + (ci & 7) * 16;
  }
  SB();
  asm volatile("s_waitcnt vmcnt(10) lgkmcnt(0)" : "+s"(tag)::"memory");
  tag += 1;
  bar();
  STAMP(1);
  rdx(0, 0);
#pragma unroll
  for (int nt = 0; nt < 5; ++nt) rdw(0, 0, nt);
  SB();
#pragma unroll
  for (int s = 0; s < 10; ++s) {
    rdx(s, 1); rdw(s, 1, 0); mm1(s, 0, 0); SB();
    rdw(s, 1, 1); rdw(s, 1, 2); mm1(s, 0, 1); SB();
    rdw(s, 1, 3); rdw(s, 1, 4); mm1(s, 0, 2); SB();
    if (s <= 6) issx(s + 3, 0);
    if (s == 7) dma<10, 0>(mkrs(pgw, 1280), GATE + 160 * w, gv, 0);
    if (s == 8) iss2(1, 1);
    if (s == 9) iss2(3, 1);
    mm1(s, 0, 3); SB();
    if (s <= 6) issx(s + 3, 1);
    if (s == 7) iss2(0, 0);
    if (s == 8) iss2(2, 0);
    if (s == 9) iss2(4, 0);
    mm1(s, 0, 4); SB();
    waitl();
    if (s < 8) waitv<7>();
    if (s == 8) waitv<6>();
    bar(); SB();
    if (s < 9) { rdx(s + 1, 0); rdw(s + 1, 0, 0); }
    mm1(s, 1, 0); SB();
    if (s < 9) { rdw(s + 1, 0, 1); rdw(s + 1, 0, 2); }
    mm1(s, 1, 1); SB();
    if (s < 9) { rdw(s + 1, 0, 3); rdw(s + 1, 0, 4); }
    if (s <= 6) issw(s + 3, 0);
    mm1(s, 1, 2); SB();
    if (s <= 6) issw(s + 3, 1);
    if (s == 7) iss2(0, 1);
    if (s == 8) iss2(2, 1);
    if (s == 9) iss2(4, 1);
    mm1(s, 1, 3); SB();
    if (s <= 6) issw(s + 3, 2);
    if (s == 7) iss2(1, 0);
    if (s == 8) iss2(3, 0);
    mm1(s, 1, 4); SB();
  }
  STAMP(2);
  if (!isB) {
    const bool isV = cs == 1;
#pragma unroll
    for (int nt = 0; nt < 5; ++nt) {
      int col = 80 * nw + 16 * nt + r, hl = col / 20, d = col - hl * 20;
      u2 lo = pk4(acc[0][nt]), hi = pk4(acc[1][nt]);
      bool sw = !isV && ((d >> 2) & 1);
      u4 v = sw ? cat(hi, lo) : cat(lo, hi);
      int ch = isV ? g ^ (((d >> 3) & 1) << 1) : g;
      int off = (hl >> 1) * (64 * 5120) + (hl & 1) * 1280 + d * 64 + ch * 16;
      __builtin_amdgcn_raw_buffer_store_b128(v, rWS, off + WS_KV + 4 * cq * (64 * 5120) + (4 * tt + mw) * 5120 + (isV ? 2560 : 0), 0, 16);
    }
    issue2(5); issue2(6); issue2(7); issue2(8);
    waitv<8 + 5 + 8>();
  } else {
    u4 k0 = cat(pk4(phi4(acc[0][0])), pk4(phi4(acc[1][0])));
    u4 k1 = cat(pk4(phi4(acc[0][1])), pk4(phi4(acc[1][1])));
    u4 kr = cat(pk4(phi4(acc[0][4])), pk4(phi4(acc[1][4])));
    u4 v0 = cat(pk4(acc[0][2]), pk4(acc[1][2]));
    u4 v1 = cat(pk4(acc[0][3]), pk4(acc[1][3]));
    u4 vr = cat(pk4(acc[0][4]), pk4(acc[1][4]));
    const unsigned one2 = 0x3F803F80u;
    if (r < 8) vr = u4{one2, one2, one2, one2};
    u4 kh;
#pragma unroll
    for (int i = 0; i < 4; ++i) kh[i] = __builtin_amdgcn_update_dpp(0u, kr[i], 0x118, 0xf, 0xf, true);
    const f4 z = {0, 0, 0, 0};
    f4 n0 = mf(k0, v0, z), n1 = mf(k1, v1, z), m2 = mf(k0, vr, z), m3 = mf(k1, vr, z);
    f4 m4 = mf(kr, v0, z), m5 = mf(kr, v1, z), m6 = mf(kh, vr, z);
    f4 n2 = (q & 1) ? m3 : m2;
    f4 n3 = g == 0 ? m4 : (g == 1 ? m5 : m6);
    int sa = SCR + w * 4096 + l * 16;
    lw128(sa, __builtin_bit_cast(u4, n0)); lw128(sa + 1024, __builtin_bit_cast(u4, n1));
    lw128(sa + 2048, __builtin_bit_cast(u4, n2)); lw128(sa + 3072, __builtin_bit_cast(u4, n3));
    waitl(); bar();
    int ra = SCR + nw * 16384 + mw * 1024 + l * 16;
    f4 s0 = __builtin_bit_cast(f4, lr128(ra)), s1 = __builtin_bit_cast(f4, lr128(ra + 4096));
    f4 s2 = __builtin_bit_cast(f4, lr128(ra + 8192)), s3 = __builtin_bit_cast(f4, lr128(ra + 12288));
    f4 sm = (s0 + s1) + (s2 + s3);
    int pb = 2 * (2 * cq + cs - 2) + nw;
    __builtin_amdgcn_raw_buffer_store_b128(__builtin_bit_cast(u4, sm), rWS, (l >> 5) * 8192 + (l & 31) * 16 +
                                           WS_KVP + pb * 65536 + mw * 16384 + tt * 512, 0, 16);
    bar();
    issue2(5); issue2(6); issue2(7); issue2(8);
    waitv<8 + 1 + 8>();
  }
  bar();
  STAMP(3);
  f4 qa[2], qb[2], qr[2];
  qa[0] = qa[1] = qb[0] = qb[1] = qr[0] = qr[1] = f4{0, 0, 0, 0};
  u4 gth[8];
  {
    const int a0 = CR + (8 * g + 2 * q) * 80 + 40 * nw + 8 * p;
    const int rb = CR + (8 * g + 2 * q) * 80 + 40 * nw + 32;
    const int r0 = rb + ((p & 1) ? 5120 + 80 : 0), r1 = rb + ((p & 1) ? 5120 : 80);
    const int ga = GATE + g * 16;
    u4 fa[2], fb[2], fr[2], fg[2], xq[2][2];
    auto rd2 = [&](int rs, int kk) {
      int o = (rs % 9) * 10240 + kk * 2560;
      fa[kk] = cat(ltr(a0 + o), ltr(a0 + o + 80));
      fb[kk] = cat(ltr(a0 + o + 5120 + 80), ltr(a0 + o + 5120));
      fr[kk] = cat(ltr(r0 + o), ltr(r1 + o));
      fg[kk] = lr128(ga + rs * 128 + kk * 64);
      if (rs < 4) {
        int xo = ((rs + 6) & 3) * 16384;
        xq[kk][0] = lr128((kk ? xa1 : xa0) + xo);
        xq[kk][1] = lr128((kk ? xa1 : xa0) + xo + 2048);
      }
    };
    auto mm2 = [&](int rs, int kk) {
      u4 x0 = rs < 4 ? xq[kk][0] : xk[rs >= 4 ? rs - 4 : 0][kk][0];
      u4 x1 = rs < 4 ? xq[kk][1] : xk[rs >= 4 ? rs - 4 : 0][kk][1];
      u4 f = r >= 8 ? fg[kk] : fr[kk];
      qa[0] = mf(fa[kk], x0, qa[0]); qa[1] = mf(fa[kk], x1, qa[1]);
      qb[0] = mf(fb[kk], x0, qb[0]); qb[1] = mf(fb[kk], x1, qb[1]);
      qr[0] = mf(f, x0, qr[0]); qr[1] = mf(f, x1, qr[1]);
    };
    rd2(0, 0);
#pragma unroll
    for (int rs = 0; rs < 10; ++rs) {
      rd2(rs, 1);
      mm2(rs, 0);
      waitl();
      if (rs <= CHK) {
        if (rs == CHK) waitvn(0);
        else if (rs == 0) waitvn(PUB == 0 ? 8 : 15);
        else if (rs < 4) waitvn(rs < PUB ? 2 * (3 - rs) + 11 : 10);
        else waitvn(rs < 8 ? 2 * (8 - rs) : 0);
        if (rs == CHK) {
          if (w == 0) {
            int t2 = tt - 2 + (l & 3);
            bool a1 = l < 8 && (l & 3) < 3 && t2 >= 0;
            int off = l < 8 ? WS_F1 + ((l >> 2) * 64 + cq * 16 + (t2 < 0 ? 0 : t2)) * 8 : WS_F2 + ((pair >> 1) * 16 + (l & 15)) * 8;
            poll(ws, off, a1 || (l >= 16 && l < 32), tag);
          }
        }
        bar();
      }
      if (rs == 0) issue2(9);
      if (rs == PUB && tid == 0) flag_store(ws, isB ? WS_F2 + ((2 * cq + cs - 2) * 16 + tt) * 8 : WS_F1 + (cs * 64 + cq * 16 + tt) * 8, tag);
      if (rs == CHK) {
        STAMP(4);
#pragma unroll
        for (int i = 0; i < 8; ++i) {
          int n = 8 * i + w;
          if (n < 60) {
            int b = n / 5, pc = n - 5 * b, gb = 4 * (tt - 2) + b;
            gb = gb < 0 ? 4 * tt : gb;
            dma<64, 1>(rWSd, b * KVPITCH + pc * 1024, l * 16, WS_KV + pair * (64 * 5120) + gb * 5120 + pc * 1024);
          }
        }
#pragma unroll
        for (int i = 0; i < 8; ++i)
          gth[i] = __builtin_amdgcn_raw_buffer_load_b128(rWS, l * 16, WS_KVP + pair * 65536 + w * 8192 + i * 1024, 16);
        if (tid < 24) {
          int b = tid >> 1;
          unsigned v = (tid & 1) ? 0x3F803F80u : (4 * (tt - 2) + b < 0 ? 0xF149F149u : 0u);
          lw128(b * KVPITCH + 5120 + (tid & 1) * 16, u4{v, v, v, v});
        }
      }
      if (rs < 9) rd2(rs + 1, 0);
      mm2(rs, 1);
    }
  }
  STAMP(5);
  {
    f4 s = __builtin_bit_cast(f4, gth[0]);
#pragma unroll
    for (int i = 1; i < 8; ++i) s += __builtin_bit_cast(f4, gth[i]);
    f4 o;
#pragma unroll
    for (int i = 0; i < 4; ++i) { u2 t = swap32(asu(s[i]), asu(s[i])); o[i] = asf(t.x) + asf(t.y); }
    if (l < 32) lw64(KF + w * 256 + l * 8, pk4(o));
  }
  float gate[2];
  u4 qf[2], pf[2];
#pragma unroll
  for (int mt = 0; mt < 2; ++mt) {
    float lg = asf(swap32(asu(qr[mt][0]), asu(qr[mt][0])).y);
    gate[mt] = __builtin_amdgcn_rcpf(1.f + __builtin_amdgcn_exp2f(-1.44269504089f * lg));
    u2 rr = pk4(qr[mt]);
    u2 up = g == 0 ? rr : (g == 1 ? u2{0x3F80u, 0u} : u2{0u, 0u});
    qf[mt] = cat(pk4(qa[mt]), up);
    u2 pr = pk4(phi4(qr[mt]));
    pf[mt] = cat(pk4(phi4(qb[mt])), g == 1 ? pr : u2{0u, 0u});
  }
  waitv<0>(); waitl(); bar();
  STAMP(6);
  const int ov = (g & 1) * 20480 + (2 * nw + (g >> 1)) * 256 + r * 16;
  const int ovr = (g & 1) * 20480 + 16384 + (pair >> 2) * 1024 + (pair & 3) * 256 + nw * 128 + r * 8;
  const int os = (8 * tt + 2 * mw) * 20480;
  auto store_o = [&](f4 m0, f4 m1, f4 e0, f4 e1, int base) {
    u2 a = pk4(m0), b = pk4(m1);
    u2 t0 = swap16(a.x, b.x), t1 = swap16(a.y, b.y);
    __builtin_amdgcn_raw_buffer_store_b128(u4{t0.x, t1.x, t0.y, t1.y}, rWS, ov + base + os + pair * 1024, 0, 16);
    u2 c = pk4(e0), d = pk4(e1);
    u2 y = u2{swap16(c.x, d.x).x, swap16(c.y, d.y).x};
    if (l < 32) __builtin_amdgcn_raw_buffer_store_b64(y, rWS, ovr, base + os, 16);
  };
  {
    int rp = r < 4 ? 8 + 4 * nw + r : 4 * nw;
    u4 a0 = cat(lr64(KF + nw * 512 + l * 8), lr64(KF + 1536 + (16 * nw + r) * 8));
    u4 a1 = cat(lr64(KF + 1024 + (16 * g + rp) * 8), lr64(KF + 1536 + (32 + 16 * nw + rp) * 8));
    const f4 z = {0, 0, 0, 0};
    f4 m[2], e[2];
#pragma unroll
    for (int mt = 0; mt < 2; ++mt) {
      m[mt] = mf(a0, pf[mt], z);
      e[mt] = mf(a1, pf[mt], z);
      float den = asf(swap16(asu(e[mt][0]), asu(e[mt][0])).y) + 1e-6f;
      float sc = (1.f - gate[mt]) * __builtin_amdgcn_rcpf(den);
      m[mt] *= sc; e[mt] *= sc;
    }
    store_o(m[0], m[1], e[0], e[1], WS_OB);
  }
  const int u = 4 * xcd + (idx >> 3), cb = idx & 7;
  const u4 rOA = mkrs(pwoa, 640 * 1280), rOB = mkrs(pwob, 640 * 1280);
  int ovm, ove;
  {
    int pr = l / 10, c = l - 10 * pr;
    int rho = 16 * (w >> 2) + 8 * (w & 1) + 4 * ((w >> 1) & 1) + pr;
    ovm = (20 * (rho >> 4) + (rho & 15)) * 1280 + 160 * cb + 16 * c;
    ove = (20 * (rho >> 2) + 16 + (rho & 3)) * 1280 + 160 * cb + 16 * c;
  }
  auto issue3 = [&](u4 rs, int kh, int i, int slot) {
    if (i < 8) dma<40, 0>(rs, slot * 5120 + w * 640, ovm, (8 * kh + i) * 51200);
    else dma<40, 0>(rs, slot * 5120 + w * 640, ove, (2 * kh + i - 8) * 204800);
  };
  {
#pragma unroll
    for (int i = 0; i < 9; ++i) { issue3(rOB, 0, i, 13 + i); issue3(rOB, 1, i, 22 + i); }
  }
  STAMP(7);
  {
    const int kb = mw * KVPITCH + nw * 1280;
    const int ka0 = kb + (4 * g + q) * 64 + p * 16 + 8 * (g & 1), ka1 = ka0 ^ 8;
    const int kr0 = (g & 1) ? mw * KVPITCH + 5120 + 8 : kb + (16 + q) * 64 + p * 16, kr1 = kr0 ^ 8;
    f4 S[2][18];
    const f4 z = {0, 0, 0, 0};
#pragma unroll
    for (int t = 0; t < 18; ++t) {
      int o = (t >> 1) * KVPITCH;
      u4 kf = cat(ltr(((t & 1) ? ka1 : ka0) + o), ltr(((t & 1) ? kr1 : kr0) + o));
      if (t < 17) S[0][t] = mf(kf, qf[0], z);
      if (t > 0) S[1][t] = mf(kf, qf[1], z);
    }
    waitv<18>();
    if (l == 0) {
      unsigned c = __hip_atomic_fetch_add((AS3 unsigned*)(size_t)MISC, 1u, __ATOMIC_RELAXED, __HIP_MEMORY_SCOPE_WORKGROUP);
      if (c == 7) flag_store(ws, WS_F3B + (tt * 16 + pair) * 8, tag);
    }
    const float C = 0.22360679775f * 1.44269504089f;
    u2 P[2][18];
    unsigned mk0 = (4 * g <= r ? 0xFFFFu : 0u) | (4 * g + 1 <= r ? 0xFFFF0000u : 0u);
    unsigned mk1 = (4 * g + 2 <= r ? 0xFFFFu : 0u) | (4 * g + 3 <= r ? 0xFFFF0000u : 0u);
#pragma unroll
    for (int mt = 0; mt < 2; ++mt) {
      f4 sm;
#pragma unroll
      for (int i = 0; i < 4; ++i) sm[i] = (4 * g + i <= r) ? S[mt][16 + mt][i] : S[mt][mt][i];
      S[mt][mt] = sm;
      float mx = fmaxf(fmaxf(sm[0], sm[1]), fmaxf(sm[2], sm[3]));
#pragma unroll
      for (int t = 1; t < 16; ++t) {
        f4 s = S[mt][mt + t];
        mx = fmaxf(fmaxf(mx, s[0]), s[1]);
        mx = fmaxf(fmaxf(mx, s[2]), s[3]);
      }
      u2 t16 = swap16(asu(mx), asu(mx));
      mx = fmaxf(asf(t16.x), asf(t16.y));
      u2 t32 = swap32(asu(mx), asu(mx));
      mx = fmaxf(asf(t32.x), asf(t32.y));
      const float nm = -mx * C;
#pragma unroll
      for (int t = 0; t < 16; ++t) {
        f4 s = S[mt][mt + t] * C + nm;
        f4 e = {__builtin_amdgcn_exp2f(s[0]), __builtin_amdgcn_exp2f(s[1]), __builtin_amdgcn_exp2f(s[2]), __builtin_amdgcn_exp2f(s[3])};
        P[mt][mt + t] = pk4(e);
      }
      u2 pm = P[mt][mt];
      P[mt][16 + mt] = u2{pm.x & mk0, pm.y & mk1};
      P[mt][mt] = u2{pm.x & ~mk0, pm.y & ~mk1};
      P[mt][17 - 17 * mt] = u2{0u, 0u};
    }
    const int vb = mw * KVPITCH + (2 + nw) * 1280;
    const int va0 = vb + r * 64 + ((g ^ (((r >> 3) & 1) << 1)) << 4);
    const int va1 = r < 4 ? vb + (16 + r) * 64 + g * 16 : mw * KVPITCH + 5136;
    f4 m[2] = {z, z}, e[2] = {z, z};
#pragma unroll
    for (int i = 0; i < 9; ++i) {
      u4 v0 = lr128(va0 + i * KVPITCH), v1 = lr128(va1 + i * KVPITCH);
#pragma unroll
      for (int mt = 0; mt < 2; ++mt) {
        u4 pp = cat(P[mt][2 * i], P[mt][2 * i + 1]);
        m[mt] = mf(v0, pp, m[mt]);
        e[mt] = mf(v1, pp, e[mt]);
      }
    }
#pragma unroll
    for (int mt = 0; mt < 2; ++mt) {
      float den = asf(swap16(asu(e[mt][0]), asu(e[mt][0])).y);
      float sc = gate[mt] * __builtin_amdgcn_rcpf(den);
      m[mt] *= sc; e[mt] *= sc;
    }
    store_o(m[0], m[1], e[0], e[1], WS_OA);
  }
  STAMP(8);
  waitv<0>();
  if (l == 0) {
    unsigned c = __hip_atomic_fetch_add((AS3 unsigned*)(size_t)(MISC + 4), 1u, __ATOMIC_RELAXED, __HIP_MEMORY_SCOPE_WORKGROUP);
    if (c == 7) flag_store(ws, WS_F3A + (tt * 16 + pair) * 8, tag);
  }
  if (w == 0) poll(ws, WS_F3B + ((u >> 1) * 16 + (l & 15)) * 8, true, tag);
  waitl(); bar();
  STAMP(9);
  {
    const int pa = (16 * (g >> 1) + 4 * (g & 1) + q) * 160 + 8 * p;
    const int pa13 = pa + (13 + 9 * nw) * 5120;
    const int ob = (4 * u + mw) * 20480;
    f4 oc[5];
#pragma unroll
    for (int i = 0; i < 5; ++i) oc[i] = f4{0, 0, 0, 0};
    u4 of[8];
    u2 orl[2], orh[2];
    auto ldo = [&](int base) {
#pragma unroll
      for (int i = 0; i < 8; ++i) of[i] = bld128(rWSd, l * 16, base + ob + (8 * nw + i) * 1024);
#pragma unroll
      for (int i = 0; i < 2; ++i) {
        int so = base + ob + 16384 + (2 * nw + i) * 1024;
        orl[i] = bld64<0>(rWSd, g * 256 + r * 8, so);
        orh[i] = bld64<128>(rWSd, g * 256 + r * 8, so);
      }
    };
    auto step = [&](int a, int i, int later) {
      u4 wfr[5];
#pragma unroll
      for (int nt = 0; nt < 5; ++nt) wfr[nt] = cat(ltr(a + 32 * nt), ltr(a + 32 * nt + 1280));
      if (i < 8) waitv_on(11 - i + later, of[i]); else waitv_on(2 * (9 - i) + later, orl[i - 8], orh[i - 8]);
      u4 o = i < 8 ? of[i] : cat(orl[i - 8], orh[i - 8]);
#pragma unroll
      for (int nt = 0; nt < 5; ++nt) oc[nt] = mf(wfr[nt], o, oc[nt]);
    };
    ldo(WS_OB);
    issue3(rOB, 0, 9, 0); issue3(rOB, 1, 9, 1);
#pragma unroll
    for (int i = 0; i < 5; ++i) { issue3(rOA, 0, i, 2 + i); issue3(rOA, 1, i, 7 + i); }
#pragma unroll
    for (int i = 0; i < 9; ++i) step(pa13 + i * 5120, i, 12);
    waitl(); waitv<10>(); bar();
    STAMP(10);
#pragma unroll
    for (int i = 5; i < 10; ++i) { issue3(rOA, 0, i, 13 + i - 5); issue3(rOA, 1, i, 22 + i - 5); }
    step(pa + nw * 5120, 9, 20);
    SB();
    waitv<0>();
    if (w == 0) poll(ws, WS_F3A + ((u >> 1) * 16 + (l & 15)) * 8, true, tag);
    bar();
    STAMP(11);
    ldo(WS_OA);
#pragma unroll
    for (int i = 0; i < 5; ++i) step(pa + (2 + 5 * nw + i) * 5120, i, 0);
#pragma unroll
    for (int i = 5; i < 10; ++i) step(pa13 + (i - 5) * 5120, i, 0);
    STAMP(12);
    const int xa = 18 * 5120 + mw * 5120 + l * 16;
    if (nw) { lw128(xa, __builtin_bit_cast(u4, oc[0])); lw128(xa + 1024, __builtin_bit_cast(u4, oc[1])); }
    else { lw128(xa + 2048, __builtin_bit_cast(u4, oc[2])); lw128(xa + 3072, __builtin_bit_cast(u4, oc[3])); lw128(xa + 4096, __builtin_bit_cast(u4, oc[4])); }
    waitl(); bar();
    char* op = out + (size_t)((64 * u + 16 * mw + r) * 1280 + 160 * cb + 8 * g);
    if (nw) {
#pragma unroll
      for (int nt = 2; nt < 5; ++nt) *(u2*)(op + 32 * nt) = pk4(oc[nt] + __builtin_bit_cast(f4, lr128(xa + 1024 * nt)));
    } else {
#pragma unroll
      for (int nt = 0; nt < 2; ++nt) *(u2*)(op + 32 * nt) = pk4(oc[nt] + __builtin_bit_cast(f4, lr128(xa + 1024 * nt)));
    }
  }
  STAMP(13);
  if (bid == 0 && w == 0) {
#pragma unroll 1
    for (int i = 0; i < 4; ++i) poll(ws, WS_F3B + (i * 64 + l) * 8, true, tag);
    if (l == 0) flag_store(ws, 0, tag);
  }
  asm volatile(".Lfk_end:" ::: "memory");
}

extern "C" void kernel_launch(void* const* d_in, const int* in_sizes, int n_in, void* d_out, int out_size, void* d_ws,
                              size_t ws_size, hipStream_t stream) {
  (void)hipFuncSetAttribute((const void*)fused_kernel, hipFuncAttributeMaxDynamicSharedMemorySize, LDS_BYTES);
  fused_kernel<<<256, 512, LDS_BYTES, stream>>>(d_in[0], d_in[1], d_in[2], d_in[4], d_in[5], d_in[6], (char*)d_out, (char*)d_ws);
}
```

```cpp
#define STAMPS
#define PROBE 0
#include <hip/hip_runtime.h>
#include <stdint.h>
typedef __bf16 bf8 __attribute__((ext_vector_type(8)));
typedef __bf16 bf2 __attribute__((ext_vector_type(2)));
typedef float f4 __attribute__((ext_vector_type(4)));
typedef float f2 __attribute__((ext_vector_type(2)));
typedef unsigned u4 __attribute__((ext_vector_type(4)));
typedef unsigned u2 __attribute__((ext_vector_type(2)));
typedef short s4 __attribute__((ext_vector_type(4)));
typedef unsigned long long u64;
#define AS3 __attribute__((address_space(3)))
typedef __amdgpu_buffer_rsrc_t rsrc_t;

constexpr int XR = 0, CR = 65536, WR = CR + 30720, GATE = 157696, MISC = 158976, KF = MISC + 64, DBGL = KF + 2048;
constexpr int SCR = CR + 5 * 10240, KVPITCH = 5184, LDS_BYTES = 163840;
constexpr int WS_F1 = 1024, WS_F2 = 2048, WS_F3B = 4096, WS_F3A = 6144, WS_DBG = 65536;
constexpr int WS_KV = 1 << 20, WS_KVP = 7 << 20, WS_OB = 8 << 20, WS_OA = 11 << 20, WS_BYTES = 16 << 20;
#define PUB 3
#define CHK 7
constexpr int SPIN_MAX = 200000;

template <int N> __device__ __forceinline__ void waitv() { asm volatile("s_waitcnt vmcnt(%0)" ::"i"(N) : "memory"); }
__device__ __forceinline__ void waitvn(int n) { asm volatile("s_waitcnt vmcnt(%0)" ::"i"(n) : "memory"); }
__device__ __forceinline__ void waitl() { asm volatile("" ::: "memory"); __builtin_amdgcn_s_waitcnt(0xC07F); asm volatile("" ::: "memory"); }
__device__ __forceinline__ void bar() { asm volatile("" ::: "memory"); __builtin_amdgcn_s_barrier(); asm volatile("" ::: "memory"); }
template <int NL, int SC1> __device__ __forceinline__ void dma(u4 rs, int lds, int voff, int soff) {
  if constexpr (NL == 64 && !SC1)
    asm volatile("s_mov_b32 m0, %0\n\ts_nop 0\n\tbuffer_load_dwordx4 %1, %2, %3 offen lds" ::"s"(lds), "v"(voff), "s"(rs), "s"(soff) : "memory");
  else if constexpr (NL == 64)
    asm volatile("s_mov_b32 m0, %0\n\ts_nop 0\n\tbuffer_load_dwordx4 %1, %2, %3 offen sc1 lds" ::"s"(lds), "v"(voff), "s"(rs), "s"(soff) : "memory");
  else if constexpr (NL >= 32)
    asm volatile("s_mov_b32 m0, %0\n\ts_mov_b32 exec_hi, %4\n\tbuffer_load_dwordx4 %1, %2, %3 offen lds\n\ts_mov_b32 exec_hi, -1" ::"s"(lds), "v"(voff), "s"(rs), "s"(soff), "i"((1 << (NL - 32)) - 1) : "memory");
  else
    asm volatile("s_mov_b32 m0, %0\n\ts_mov_b64 exec, %4\n\tbuffer_load_dwordx4 %1, %2, %3 offen lds\n\ts_mov_b64 exec, -1" ::"s"(lds), "v"(voff), "s"(rs), "s"(soff), "i"((1 << NL) - 1) : "memory");
}
__device__ __forceinline__ u4 mkrs(const void* p, unsigned bytes) {
  u64 a = (u64)p;
  return u4{(unsigned)a, (unsigned)(a >> 32) & 0xffffu, bytes, 0x00020000u};
}
__device__ __forceinline__ u4 bld128(u4 rs, int voff, int soff) {
  u4 d; asm volatile("buffer_load_dwordx4 %0, %1, %2, %3 offen sc1" : "=v"(d) : "v"(voff), "s"(rs), "s"(soff) : "memory"); return d;
}
template <int OFF> __device__ __forceinline__ u2 bld64(u4 rs, int voff, int soff) {
  u2 d; asm volatile("buffer_load_dwordx2 %0, %1, %2, %3 offen offset:%4 sc1" : "=v"(d) : "v"(voff), "s"(rs), "s"(soff), "i"(OFF) : "memory"); return d;
}
__device__ __forceinline__ void waitv_on(int n, u4& a) { asm volatile("s_waitcnt vmcnt(%1)" : "+v"(a) : "i"(n) : "memory"); }
__device__ __forceinline__ void waitv_on(int n, u2& a, u2& b) { asm volatile("s_waitcnt vmcnt(%2)" : "+v"(a), "+v"(b) : "i"(n) : "memory"); }
#define SB() __builtin_amdgcn_sched_barrier(0)
__device__ __forceinline__ u4 lr128(int a) { return *(const AS3 u4*)(size_t)a; }
__device__ __forceinline__ u2 lr64(int a) { return *(const AS3 u2*)(size_t)a; }
__device__ __forceinline__ void lw128(int a, u4 v) { *(AS3 u4*)(size_t)a = v; }
__device__ __forceinline__ void lw64(int a, u2 v) { *(AS3 u2*)(size_t)a = v; }
__device__ __forceinline__ u2 ltr(int a) {
  s4 v = __builtin_amdgcn_ds_read_tr16_b64_v4i16((AS3 s4*)(size_t)a);
  return __builtin_bit_cast(u2, v);
}
__device__ __forceinline__ u4 cat(u2 a, u2 b) { return u4{a.x, a.y, b.x, b.y}; }
__device__ __forceinline__ f4 mf(u4 a, u4 b, f4 c) {
  return __builtin_amdgcn_mfma_f32_16x16x32_bf16(__builtin_bit_cast(bf8, a), __builtin_bit_cast(bf8, b), c, 0, 0, 0);
}
__device__ __forceinline__ unsigned pk(float a, float b) {
  f2 v = {a, b};
  return __builtin_bit_cast(unsigned, __builtin_convertvector(v, bf2));
}
__device__ __forceinline__ u2 pk4(f4 v) { return u2{pk(v.x, v.y), pk(v.z, v.w)}; }
__device__ __forceinline__ float phi(float t) { return (t > 0.f ? t + 1.f : __builtin_amdgcn_exp2f(t * 1.44269504089f)) + 1e-6f; }
__device__ __forceinline__ f4 phi4(f4 v) { return f4{phi(v.x), phi(v.y), phi(v.z), phi(v.w)}; }
__device__ __forceinline__ u2 swap16(unsigned a, unsigned b) {
  auto r = __builtin_amdgcn_permlane16_swap(a, b, false, false);
  return u2{r[0], r[1]};
}
__device__ __forceinline__ u2 swap32(unsigned a, unsigned b) {
  auto r = __builtin_amdgcn_permlane32_swap(a, b, false, false);
  return u2{r[0], r[1]};
}
__device__ __forceinline__ float asf(unsigned x) { return __builtin_bit_cast(float, x); }
__device__ __forceinline__ unsigned asu(float x) { return __builtin_bit_cast(unsigned, x); }
__device__ __forceinline__ void flag_store(char* ws, int off, u64 tag) {
  __hip_atomic_store((u64*)(ws + off), tag, __ATOMIC_RELAXED, __HIP_MEMORY_SCOPE_AGENT);
}
__device__ __forceinline__ void poll(char* ws, int off, bool act, u64 tag) {
#pragma unroll 1
  for (int it = 0; it < SPIN_MAX; ++it) {
    u64 v = __hip_atomic_load((u64*)(ws + off), __ATOMIC_RELAXED, __HIP_MEMORY_SCOPE_AGENT);
    if (__builtin_amdgcn_ballot_w64(act && v != tag) == 0) break;
    __builtin_amdgcn_s_sleep(2);
  }
  asm volatile("" ::: "memory");
}
#define STAMP(i) do { if (w == STAMPW) { unsigned t_ = (unsigned)__builtin_amdgcn_s_memrealtime(); *(AS3 unsigned*)(size_t)(DBGL + 4 * (i)) = t_; } } while (0)
#define STAMPW 0

__global__ __launch_bounds__(512, 2) void fused_kernel(const void* __restrict__ px, const void* __restrict__ pwa,
                                                       const void* __restrict__ pwoa, const void* __restrict__ pwb,
                                                       const void* __restrict__ pwob, const void* __restrict__ pgw,
                                                       char* __restrict__ out, char* __restrict__ ws) {
  const int tid = threadIdx.x, l = tid & 63, w = __builtin_amdgcn_readfirstlane(tid >> 6);
  const int bid = blockIdx.x, xcd = bid & 7, idx = bid >> 3;
  {
    u64 pc; unsigned sz;
    asm volatile("s_getpc_b64 %0\n.Lfk_start:\n s_mov_b32 %1, .Lfk_end-.Lfk_start" : "=s"(pc), "=s"(sz));
    unsigned o = (unsigned)((((2 * idx + w) & 7) * 64 + l) * 128);
    if (w < 2 && o < sz)
      asm volatile("s_mov_b32 m0, %2\n\ts_nop 0\n\tglobal_load_lds_dword %0, %1" ::"v"(o), "s"(pc), "s"(DBGL + 256) : "memory");
  }
  const int r = l & 15, g = l >> 4, q = r >> 2, p = r & 3, mw = w & 3, nw = w >> 2;
  const int cq = xcd >> 1, tt = 8 * (xcd & 1) + (idx & 7), cs = idx >> 3;
  const bool isB = cs >= 2;
  const int pair = 4 * cq + 2 * (cs & 1) + ((cs < 2 ? 1 : 0) ^ (cq & 1));
  const int rot = (idx * 3 + (idx >> 3)) % 10;
  const u4 rX = mkrs(px, 2048 * 1280), rWA = mkrs(pwa, 640 * 3840), rWB = mkrs(pwb, 640 * 3840);
  const u4 rW1 = mkrs(isB ? pwb : pwa, 640 * 3840), rWSd = mkrs(ws, WS_BYTES);
  const rsrc_t rWS = __builtin_amdgcn_make_buffer_rsrc((void*)ws, 0, WS_BYTES, 0x00020000);
  STAMP(0);
  int xv0, xv1, wv[3];
  {
    int row = 16 * w + (l >> 3);
    xv0 = row * 1280 + (((l & 7) ^ ((row >> 1) & 7)) << 4);
    xv1 = (xv0 ^ 64) + 8 * 1280;
    const int base0 = isB ? 1280 + 160 * (2 * cq + cs - 2) : 1280 + 1280 * cs + 320 * cq;
    const int delta = isB ? 1120 : 0;
#pragma unroll
    for (int i = 0; i < 3; ++i) {
      int ci = 64 * i + l, rw = ci / 20, pos = ci - rw * 20;
      int t = pos ^ (2 * (w & 1));
      int c = t >= 10 ? t ^ 1 : t;
      wv[i] = (8 * w + rw) * 3840 + base0 + 16 * c + (c >= 10 ? delta : 0);
    }
  }
  const int xs0 = tt * (128 * 1280);
  auto kchunk = [&](int s) { int k = s + rot; return k >= 10 ? k - 10 : k; };
  auto issx = [&](int s, int i) { dma<64, 0>(rX, XR + (s & 3) * 16384 + w * 2048 + i * 1024, i ? xv1 : xv0, xs0 + kchunk(s) * 128); };
  auto issw = [&](int s, int i) {
    int wd = WR + ((s + 2) % 3) * 20480 + w * 2560 + i * 1024, so = kchunk(s) * 245760;
    if (i < 2) dma<64, 0>(rW1, wd, wv[i], so); else dma<32, 0>(rW1, wd, wv[2], so);
  };
#pragma unroll
  for (int s = 0; s < 3; ++s) { issx(s, 0); issx(s, 1); issw(s, 0); issw(s, 1); issw(s, 2); }
  SB();
  u64 tag;
  asm volatile("s_load_dwordx2 %0, %1, 0x0" : "=s"(tag) : "s"(ws) : "memory");
  int qva, qvb;
  {
    int pr = l / 5, c = l - pr * 5;
    int kb = 8 * w + (pr >> 1);
    qva = (kb + 4 * (pr & 1)) * 3840 + 80 * pair + 16 * c;
    qvb = (kb + 4 * ((pr & 1) ^ 1)) * 3840 + 80 * pair + 16 * c;
  }
  auto iss2 = [&](int rs, int i) {
    dma<40, 0>(i ? rWB : rWA, CR + (rs % 9) * 10240 + w * 640 + i * 5120, i ? qvb : qva, kchunk((rs + 6) % 10) * 245760);
  };
  auto issue2 = [&](int rs) { iss2(rs, 0); iss2(rs, 1); };
  int wa[5];
#pragma unroll
  for (int nt = 0; nt < 5; ++nt) {
    int byteA = 160 * nw + 32 * nt + 8 * p;
    int byteB = nt < 4 ? 160 * (nt >> 1) + 80 * nw + 40 * (nt & 1) + 8 * p : 160 * (p >> 1) + 80 * nw + 40 * (p & 1) + 32;
    int by = isB ? byteB : byteA;
    if (by >= 160) by ^= 16;
    by ^= (g & 1) << 5;
    wa[nt] = WR + (8 * g + q) * 320 + by;
  }
  const int xa0 = XR + (32 * mw + r) * 128 + ((g ^ (r >> 1)) << 4), xa1 = xa0 ^ 64;
  if (tid < 4) *(AS3 unsigned*)(size_t)(MISC + 4 * tid) = 0;

  u4 xk[10][2][2];
  u4 wf[2][5];
  f4 acc[2][5];
#pragma unroll
  for (int i = 0; i < 5; ++i) acc[0][i] = acc[1][i] = f4{0, 0, 0, 0};
  auto rdx = [&](int s, int kk) {
    int xo = (s & 3) * 16384;
    xk[s][kk][0] = lr128((kk ? xa1 : xa0) + xo);
    xk[s][kk][1] = lr128((kk ? xa1 : xa0) + xo + 2048);
  };
  auto rdw = [&](int s, int kk, int nt) {
    int wo = ((s + 2) % 3) * 20480 + kk * 10240;
    wf[kk][nt] = cat(ltr(wa[nt] + wo), ltr(wa[nt] + wo + 1280));
  };
  auto mm1 = [&](int s, int kk, int nt) {
    acc[0][nt] = mf(xk[s][kk][0], wf[kk][nt], acc[0][nt]);
    acc[1][nt] = mf(xk[s][kk][1], wf[kk][nt], acc[1][nt]);
  };
  int gv;
  {
    int ci = 10 * w + l, kc = (ci >> 3) + 6 + rot;
    kc = kc >= 20 ? kc - 20 : (kc >= 10 ? kc - 10 : kc);
    gv = kc * 128 + (ci & 7) * 16;
  }
  SB();
  asm volatile("s_waitcnt vmcnt(10) lgkmcnt(0)" : "+s"(tag)::"memory");
  tag += 1;
  bar();
  STAMP(1);
  rdx(0, 0);
#pragma unroll
  for (int nt = 0; nt < 5; ++nt) rdw(0, 0, nt);
  SB();
#pragma unroll
  for (int s = 0; s < 10; ++s) {
    rdx(s, 1); rdw(s, 1, 0); mm1(s, 0, 0); SB();
    rdw(s, 1, 1); rdw(s, 1, 2); mm1(s, 0, 1); SB();
    rdw(s, 1, 3); rdw(s, 1, 4); mm1(s, 0, 2); SB();
    if (s <= 6) issx(s + 3, 0);
    if (s == 7) dma<10, 0>(mkrs(pgw, 1280), GATE + 160 * w, gv, 0);
    if (s == 8) iss2(1, 1);
    if (s == 9) iss2(3, 1);
    mm1(s, 0, 3); SB();
    if (s <= 6) issx(s + 3, 1);
    if (s == 7) iss2(0, 0);
    if (s == 8) iss2(2, 0);
    if (s == 9) iss2(4, 0);
    mm1(s, 0, 4); SB();
    waitl();
    if (s < 8) waitv<7>();
    if (s == 8) waitv<6>();
    bar(); SB();
    if (s < 9) { rdx(s + 1, 0); rdw(s + 1, 0, 0); }
    mm1(s, 1, 0); SB();
    if (s < 9) { rdw(s + 1, 0, 1); rdw(s + 1, 0, 2); }
    mm1(s, 1, 1); SB();
    if (s < 9) { rdw(s + 1, 0, 3); rdw(s + 1, 0, 4); }
    if (s <= 6) issw(s + 3, 0);
    mm1(s, 1, 2); SB();
    if (s <= 6) issw(s + 3, 1);
    if (s == 7) iss2(0, 1);
    if (s == 8) iss2(2, 1);
    if (s == 9) iss2(4, 1);
    mm1(s, 1, 3); SB();
    if (s <= 6) issw(s + 3, 2);
    if (s == 7) iss2(1, 0);
    if (s == 8) iss2(3, 0);
    mm1(s, 1, 4); SB();
  }
  STAMP(2);
  if (!isB) {
    const bool isV = cs == 1;
#pragma unroll
    for (int nt = 0; nt < 5; ++nt) {
      int col = 80 * nw + 16 * nt + r, hl = col / 20, d = col - hl * 20;
      u2 lo = pk4(acc[0][nt]), hi = pk4(acc[1][nt]);
      bool sw = !isV && ((d >> 2) & 1);
      u4 v = sw ? cat(hi, lo) : cat(lo, hi);
      int ch = isV ? g ^ (((d >> 3) & 1) << 1) : g;
      int off = (hl >> 1) * (64 * 5120) + (hl & 1) * 1280 + d * 64 + ch * 16;
      __builtin_amdgcn_raw_buffer_store_b128(v, rWS, off + WS_KV + 4 * cq * (64 * 5120) + (4 * tt + mw) * 5120 + (isV ? 2560 : 0), 0, 16);
    }
    issue2(5); issue2(6); issue2(7); issue2(8);
    waitv<8 + 5 + 8>();
  } else {
    u4 k0 = cat(pk4(phi4(acc[0][0])), pk4(phi4(acc[1][0])));
    u4 k1 = cat(pk4(phi4(acc[0][1])), pk4(phi4(acc[1][1])));
    u4 kr = cat(pk4(phi4(acc[0][4])), pk4(phi4(acc[1][4])));
    u4 v0 = cat(pk4(acc[0][2]), pk4(acc[1][2]));
    u4 v1 = cat(pk4(acc[0][3]), pk4(acc[1][3]));
    u4 vr = cat(pk4(acc[0][4]), pk4(acc[1][4]));
    const unsigned one2 = 0x3F803F80u;
    if (r < 8) vr = u4{one2, one2, one2, one2};
    u4 kh;
#pragma unroll
    for (int i = 0; i < 4; ++i) kh[i] = __builtin_amdgcn_update_dpp(0u, kr[i], 0x118, 0xf, 0xf, true);
    const f4 z = {0, 0, 0, 0};
    f4 n0 = mf(k0, v0, z), n1 = mf(k1, v1, z), m2 = mf(k0, vr, z), m3 = mf(k1, vr, z);
    f4 m4 = mf(kr, v0, z), m5 = mf(kr, v1, z), m6 = mf(kh, vr, z);
    f4 n2 = (q & 1) ? m3 : m2;
    f4 n3 = g == 0 ? m4 : (g == 1 ? m5 : m6);
    int sa = SCR + w * 4096 + l * 16;
    lw128(sa, __builtin_bit_cast(u4, n0)); lw128(sa + 1024, __builtin_bit_cast(u4, n1));
    lw128(sa + 2048, __builtin_bit_cast(u4, n2)); lw128(sa + 3072, __builtin_bit_cast(u4, n3));
    waitl(); bar();
    int ra = SCR + nw * 16384 + mw * 1024 + l * 16;
    f4 s0 = __builtin_bit_cast(f4, lr128(ra)), s1 = __builtin_bit_cast(f4, lr128(ra + 4096));
    f4 s2 = __builtin_bit_cast(f4, lr128(ra + 8192)), s3 = __builtin_bit_cast(f4, lr128(ra + 12288));
    f4 sm = (s0 + s1) + (s2 + s3);
    int pb = 2 * (2 * cq + cs - 2) + nw;
    __builtin_amdgcn_raw_buffer_store_b128(__builtin_bit_cast(u4, sm), rWS, (l >> 5) * 8192 + (l & 31) * 16 +
                                           WS_KVP + pb * 65536 + mw * 16384 + tt * 512, 0, 16);
    bar();
    issue2(5); issue2(6); issue2(7); issue2(8);
    waitv<8 + 1 + 8>();
  }
  bar();
  STAMP(3);
  f4 qa[2], qb[2], qr[2];
  qa[0] = qa[1] = qb[0] = qb[1] = qr[0] = qr[1] = f4{0, 0, 0, 0};
  u4 gth[8];
  {
    const int a0 = CR + (8 * g + 2 * q) * 80 + 40 * nw + 8 * p;
    const int rb = CR + (8 * g + 2 * q) * 80 + 40 * nw + 32;
    const int r0 = rb + ((p & 1) ? 5120 + 80 : 0), r1 = rb + ((p & 1) ? 5120 : 80);
    const int ga = GATE + g * 16;
    u4 fa[2], fb[2], fr[2], fg[2], xq[2][2];
    auto rd2 = [&](int rs, int kk) {
      int o = (rs % 9) * 10240 + kk * 2560;
      fa[kk] = cat(ltr(a0 + o), ltr(a0 + o + 80));
      fb[kk] = cat(ltr(a0 + o + 5120 + 80), ltr(a0 + o + 5120));
      fr[kk] = cat(ltr(r0 + o), ltr(r1 + o));
      fg[kk] = lr128(ga + rs * 128 + kk * 64);
      if (rs < 4) {
        int xo = ((rs + 6) & 3) * 16384;
        xq[kk][0] = lr128((kk ? xa1 : xa0) + xo);
        xq[kk][1] = lr128((kk ? xa1 : xa0) + xo + 2048);
      }
    };
    auto mm2 = [&](int rs, int kk) {
      u4 x0 = rs < 4 ? xq[kk][0] : xk[rs >= 4 ? rs - 4 : 0][kk][0];
      u4 x1 = rs < 4 ? xq[kk][1] : xk[rs >= 4 ? rs - 4 : 0][kk][1];
      u4 f = r >= 8 ? fg[kk] : fr[kk];
      qa[0] = mf(fa[kk], x0, qa[0]); qa[1] = mf(fa[kk], x1, qa[1]);
      qb[0] = mf(fb[kk], x0, qb[0]); qb[1] = mf(fb[kk], x1, qb[1]);
      qr[0] = mf(f, x0, qr[0]); qr[1] = mf(f, x1, qr[1]);
    };
    rd2(0, 0);
#pragma unroll
    for (int rs = 0; rs < 10; ++rs) {
      rd2(rs, 1);
      mm2(rs, 0);
      waitl();
      if (rs <= CHK) {
        if (rs == CHK) waitvn(0);
        else if (rs == 0) waitvn(PUB == 0 ? 8 : 15);
        else if (rs < 4) waitvn(rs < PUB ? 2 * (3 - rs) + 11 : 10);
        else waitvn(rs < 8 ? 2 * (8 - rs) : 0);
        if (rs == CHK) {
          if (w == 0) {
            int t2 = tt - 2 + (l & 3);
            bool a1 = l < 8 && (l & 3) < 3 && t2 >= 0;
            int off = l < 8 ? WS_F1 + ((l >> 2) * 64 + cq * 16 + (t2 < 0 ? 0 : t2)) * 8 : WS_F2 + ((pair >> 1) * 16 + (l & 15)) * 8;
            poll(ws, off, a1 || (l >= 16 && l < 32), tag);
          }
        }
        bar();
      }
      if (rs == 0) issue2(9);
      if (rs == PUB && tid == 0) flag_store(ws, isB ? WS_F2 + ((2 * cq + cs - 2) * 16 + tt) * 8 : WS_F1 + (cs * 64 + cq * 16 + tt) * 8, tag);
      if (rs == CHK) {
        STAMP(4);
#pragma unroll
        for (int i = 0; i < 8; ++i) {
          int n = 8 * i + w;
          if (n < 60) {
            int b = n / 5, pc = n - 5 * b, gb = 4 * (tt - 2) + b;
            gb = gb < 0 ? 4 * tt : gb;
            dma<64, 1>(rWSd, b * KVPITCH + pc * 1024, l * 16, WS_KV + pair * (64 * 5120) + gb * 5120 + pc * 1024);
          }
        }
#pragma unroll
        for (int i = 0; i < 8; ++i)
          gth[i] = __builtin_amdgcn_raw_buffer_load_b128(rWS, l * 16, WS_KVP + pair * 65536 + w * 8192 + i * 1024, 16);
        if (tid < 24) {
          int b = tid >> 1;
          unsigned v = (tid & 1) ? 0x3F803F80u : (4 * (tt - 2) + b < 0 ? 0xF149F149u : 0u);
          lw128(b * KVPITCH + 5120 + (tid & 1) * 16, u4{v, v, v, v});
        }
      }
      if (rs < 9) rd2(rs + 1, 0);
      mm2(rs, 1);
    }
  }
  STAMP(5);
  {
    f4 s = __builtin_bit_cast(f4, gth[0]);
#pragma unroll
    for (int i = 1; i < 8; ++i) s += __builtin_bit_cast(f4, gth[i]);
    f4 o;
#pragma unroll
    for (int i = 0; i < 4; ++i) { u2 t = swap32(asu(s[i]), asu(s[i])); o[i] = asf(t.x) + asf(t.y); }
    if (l < 32) lw64(KF + w * 256 + l * 8, pk4(o));
  }
  float gate[2];
  u4 qf[2], pf[2];
#pragma unroll
  for (int mt = 0; mt < 2; ++mt) {
    float lg = asf(swap32(asu(qr[mt][0]), asu(qr[mt][0])).y);
    gate[mt] = __builtin_amdgcn_rcpf(1.f + __builtin_amdgcn_exp2f(-1.44269504089f * lg));
    u2 rr = pk4(qr[mt]);
    u2 up = g == 0 ? rr : (g == 1 ? u2{0x3F80u, 0u} : u2{0u, 0u});
    qf[mt] = cat(pk4(qa[mt]), up);
    u2 pr = pk4(phi4(qr[mt]));
    pf[mt] = cat(pk4(phi4(qb[mt])), g == 1 ? pr : u2{0u, 0u});
  }
  waitv<0>(); waitl(); bar();
  STAMP(6);
  const int ov = (g & 1) * 20480 + (2 * nw + (g >> 1)) * 256 + r * 16;
  const int ovr = (g & 1) * 20480 + 16384 + (pair >> 2) * 1024 + (pair & 3) * 256 + nw * 128 + r * 8;
  const int os = (8 * tt + 2 * mw) * 20480;
  auto store_o = [&](f4 m0, f4 m1, f4 e0, f4 e1, int base) {
    u2 a = pk4(m0), b = pk4(m1);
    u2 t0 = swap16(a.x, b.x), t1 = swap16(a.y, b.y);
    __builtin_amdgcn_raw_buffer_store_b128(u4{t0.x, t1.x, t0.y, t1.y}, rWS, ov + base + os + pair * 1024, 0, 16);
    u2 c = pk4(e0), d = pk4(e1);
    u2 y = u2{swap16(c.x, d.x).x, swap16(c.y, d.y).x};
    if (l < 32) __builtin_amdgcn_raw_buffer_store_b64(y, rWS, ovr, base + os, 16);
  };
  {
    int rp = r < 4 ? 8 + 4 * nw + r : 4 * nw;
    u4 a0 = cat(lr64(KF + nw * 512 + l * 8), lr64(KF + 1536 + (16 * nw + r) * 8));
    u4 a1 = cat(lr64(KF + 1024 + (16 * g + rp) * 8), lr64(KF + 1536 + (32 + 16 * nw + rp) * 8));
    const f4 z = {0, 0, 0, 0};
    f4 m[2], e[2];
#pragma unroll
    for (int mt = 0; mt < 2; ++mt) {
      m[mt] = mf(a0, pf[mt], z);
      e[mt] = mf(a1, pf[mt], z);
      float den = asf(swap16(asu(e[mt][0]), asu(e[mt][0])).y) + 1e-6f;
      float sc = (1.f - gate[mt]) * __builtin_amdgcn_rcpf(den);
      m[mt] *= sc; e[mt] *= sc;
    }
    store_o(m[0], m[1], e[0], e[1], WS_OB);
  }
  const int u = 4 * xcd + (idx >> 3), cb = idx & 7;
  const u4 rOA = mkrs(pwoa, 640 * 1280), rOB = mkrs(pwob, 640 * 1280);
  int ovm, ove;
  {
    int pr = l / 10, c = l - 10 * pr;
    int rho = 16 * (w >> 2) + 8 * (w & 1) + 4 * ((w >> 1) & 1) + pr;
    ovm = (20 * (rho >> 4) + (rho & 15)) * 1280 + 160 * cb + 16 * c;
    ove = (20 * (rho >> 2) + 16 + (rho & 3)) * 1280 + 160 * cb + 16 * c;
  }
  auto issue3 = [&](u4 rs, int kh, int i, int slot) {
    if (i < 8) dma<40, 0>(rs, slot * 5120 + w * 640, ovm, (8 * kh + i) * 51200);
    else dma<40, 0>(rs, slot * 5120 + w * 640, ove, (2 * kh + i - 8) * 204800);
  };
  {
#pragma unroll
    for (int i = 0; i < 9; ++i) { issue3(rOB, 0, i, 13 + i); issue3(rOB, 1, i, 22 + i); }
  }
  STAMP(7);
  {
    const int kb = mw * KVPITCH + nw * 1280;
    const int ka0 = kb + (4 * g + q) * 64 + p * 16 + 8 * (g & 1), ka1 = ka0 ^ 8;
    const int kr0 = (g & 1) ? mw * KVPITCH + 5120 + 8 : kb + (16 + q) * 64 + p * 16, kr1 = kr0 ^ 8;
    f4 S[2][18];
    const f4 z = {0, 0, 0, 0};
#pragma unroll
    for (int t = 0; t < 18; ++t) {
      int o = (t >> 1) * KVPITCH;
      u4 kf = cat(ltr(((t & 1) ? ka1 : ka0) + o), ltr(((t & 1) ? kr1 : kr0) + o));
      if (t < 17) S[0][t] = mf(kf, qf[0], z);
      if (t > 0) S[1][t] = mf(kf, qf[1], z);
    }
    waitv<18>();
    if (l == 0) {
      unsigned c = __hip_atomic_fetch_add((AS3 unsigned*)(size_t)MISC, 1u, __ATOMIC_RELAXED, __HIP_MEMORY_SCOPE_WORKGROUP);
      if (c == 7) flag_store(ws, WS_F3B + (tt * 16 + pair) * 8, tag);
    }
    const float C = 0.22360679775f * 1.44269504089f;
    u2 P[2][18];
    unsigned mk0 = (4 * g <= r ? 0xFFFFu : 0u) | (4 * g + 1 <= r ? 0xFFFF0000u : 0u);
    unsigned mk1 = (4 * g + 2 <= r ? 0xFFFFu : 0u) | (4 * g + 3 <= r ? 0xFFFF0000u : 0u);
#pragma unroll
    for (int mt = 0; mt < 2; ++mt) {
      f4 sm;
#pragma unroll
      for (int i = 0; i < 4; ++i) sm[i] = (4 * g + i <= r) ? S[mt][16 + mt][i] : S[mt][mt][i];
      S[mt][mt] = sm;
      float mx = fmaxf(fmaxf(sm[0], sm[1]), fmaxf(sm[2], sm[3]));
#pragma unroll
      for (int t = 1; t < 16; ++t) {
        f4 s = S[mt][mt + t];
        mx = fmaxf(fmaxf(mx, s[0]), s[1]);
        mx = fmaxf(fmaxf(mx, s[2]), s[3]);
      }
      u2 t16 = swap16(asu(mx), asu(mx));
      mx = fmaxf(asf(t16.x), asf(t16.y));
      u2 t32 = swap32(asu(mx), asu(mx));
      mx = fmaxf(asf(t32.x), asf(t32.y));
      const float nm = -mx * C;
#pragma unroll
      for (int t = 0; t < 16; ++t) {
        f4 s = S[mt][mt + t] * C + nm;
        f4 e = {__builtin_amdgcn_exp2f(s[0]), __builtin_amdgcn_exp2f(s[1]), __builtin_amdgcn_exp2f(s[2]), __builtin_amdgcn_exp2f(s[3])};
        P[mt][mt + t] = pk4(e);
      }
      u2 pm = P[mt][mt];
      P[mt][16 + mt] = u2{pm.x & mk0, pm.y & mk1};
      P[mt][mt] = u2{pm.x & ~mk0, pm.y & ~mk1};
      P[mt][17 - 17 * mt] = u2{0u, 0u};
    }
    const int vb = mw * KVPITCH + (2 + nw) * 1280;
    const int va0 = vb + r * 64 + ((g ^ (((r >> 3) & 1) << 1)) << 4);
    const int va1 = r < 4 ? vb + (16 + r) * 64 + g * 16 : mw * KVPITCH + 5136;
    f4 m[2] = {z, z}, e[2] = {z, z};
#pragma unroll
    for (int i = 0; i < 9; ++i) {
      u4 v0 = lr128(va0 + i * KVPITCH), v1 = lr128(va1 + i * KVPITCH);
#pragma unroll
      for (int mt = 0; mt < 2; ++mt) {
        u4 pp = cat(P[mt][2 * i], P[mt][2 * i + 1]);
        m[mt] = mf(v0, pp, m[mt]);
        e[mt] = mf(v1, pp, e[mt]);
      }
    }
#pragma unroll
    for (int mt = 0; mt < 2; ++mt) {
      float den = asf(swap16(asu(e[mt][0]), asu(e[mt][0])).y);
      float sc = gate[mt] * __builtin_amdgcn_rcpf(den);
      m[mt] *= sc; e[mt] *= sc;
    }
    store_o(m[0], m[1], e[0], e[1], WS_OA);
  }
  STAMP(8);
  waitv<0>();
  if (l == 0) {
    unsigned c = __hip_atomic_fetch_add((AS3 unsigned*)(size_t)(MISC + 4), 1u, __ATOMIC_RELAXED, __HIP_MEMORY_SCOPE_WORKGROUP);
    if (c == 7) flag_store(ws, WS_F3A + (tt * 16 + pair) * 8, tag);
  }
  if (w == 0) poll(ws, WS_F3B + ((u >> 1) * 16 + (l & 15)) * 8, true, tag);
  waitl(); bar();
  STAMP(9);
  {
    const int pa = (16 * (g >> 1) + 4 * (g & 1) + q) * 160 + 8 * p;
    const int pa13 = pa + (13 + 9 * nw) * 5120;
    const int ob = (4 * u + mw) * 20480;
    f4 oc[5];
#pragma unroll
    for (int i = 0; i < 5; ++i) oc[i] = f4{0, 0, 0, 0};
    u4 of[8];
    u2 orl[2], orh[2];
    auto ldo = [&](int base) {
#pragma unroll
      for (int i = 0; i < 8; ++i) of[i] = bld128(rWSd, l * 16, base + ob + (8 * nw + i) * 1024);
#pragma unroll
      for (int i = 0; i < 2; ++i) {
        int so = base + ob + 16384 + (2 * nw + i) * 1024;
        orl[i] = bld64<0>(rWSd, g * 256 + r * 8, so);
        orh[i] = bld64<128>(rWSd, g * 256 + r * 8, so);
      }
    };
    auto step = [&](int a, int i, int later) {
      u4 wfr[5];
#pragma unroll
      for (int nt = 0; nt < 5; ++nt) wfr[nt] = cat(ltr(a + 32 * nt), ltr(a + 32 * nt + 1280));
      if (i < 8) waitv_on(11 - i + later, of[i]); else waitv_on(2 * (9 - i) + later, orl[i - 8], orh[i - 8]);
      u4 o = i < 8 ? of[i] : cat(orl[i - 8], orh[i - 8]);
#pragma unroll
      for (int nt = 0; nt < 5; ++nt) oc[nt] = mf(wfr[nt], o, oc[nt]);
    };
    ldo(WS_OB);
    issue3(rOB, 0, 9, 0); issue3(rOB, 1, 9, 1);
#pragma unroll
    for (int i = 0; i < 5; ++i) { issue3(rOA, 0, i, 2 + i); issue3(rOA, 1, i, 7 + i); }
#pragma unroll
    for (int i = 0; i < 9; ++i) step(pa13 + i * 5120, i, 12);
    waitl(); waitv<10>(); bar();
    STAMP(10);
#pragma unroll
    for (int i = 5; i < 10; ++i) { issue3(rOA, 0, i, 13 + i - 5); issue3(rOA, 1, i, 22 + i - 5); }
    step(pa + nw * 5120, 9, 20);
    SB();
    waitv<0>();
    if (w == 0) poll(ws, WS_F3A + ((u >> 1) * 16 + (l & 15)) * 8, true, tag);
    bar();
    STAMP(11);
    ldo(WS_OA);
#pragma unroll
    for (int i = 0; i < 5; ++i) step(pa + (2 + 5 * nw + i) * 5120, i, 0);
#pragma unroll
    for (int i = 5; i < 10; ++i) step(pa13 + (i - 5) * 5120, i, 0);
    STAMP(12);
    const int xa = 18 * 5120 + mw * 5120 + l * 16;
    if (nw) { lw128(xa, __builtin_bit_cast(u4, oc[0])); lw128(xa + 1024, __builtin_bit_cast(u4, oc[1])); }
    else { lw128(xa + 2048, __builtin_bit_cast(u4, oc[2])); lw128(xa + 3072, __builtin_bit_cast(u4, oc[3])); lw128(xa + 4096, __builtin_bit_cast(u4, oc[4])); }
    waitl(); bar();
    char* op = out + (size_t)((64 * u + 16 * mw + r) * 1280 + 160 * cb + 8 * g);
    if (nw) {
#pragma unroll
      for (int nt = 2; nt < 5; ++nt) *(u2*)(op + 32 * nt) = pk4(oc[nt] + __builtin_bit_cast(f4, lr128(xa + 1024 * nt)));
    } else {
#pragma unroll
      for (int nt = 0; nt < 2; ++nt) *(u2*)(op + 32 * nt) = pk4(oc[nt] + __builtin_bit_cast(f4, lr128(xa + 1024 * nt)));
    }
  }
  STAMP(13);
  if (bid == 0 && w == 0) {
#pragma unroll 1
    for (int i = 0; i < 4; ++i) poll(ws, WS_F3B + (i * 64 + l) * 8, true, tag);
    if (l == 0) flag_store(ws, 0, tag);
  }
  if (w == STAMPW && bid == 37) {
    waitl();
    unsigned d = *(AS3 unsigned*)(size_t)(DBGL + 4 * PROBE) - *(AS3 unsigned*)(size_t)(DBGL);
    d = __builtin_amdgcn_readfirstlane(d);
    if (d > 10000u) d = 10000u;
    unsigned t0 = (unsigned)__builtin_amdgcn_s_memrealtime();
#pragma unroll 1
    while ((unsigned)__builtin_amdgcn_s_memrealtime() - t0 < d * 16u) __builtin_amdgcn_s_sleep(8);
  }
  if (w == STAMPW) { waitl(); if (l < 16) *(unsigned*)(ws + WS_DBG + bid * 64 + l * 4) = *(AS3 unsigned*)(size_t)(DBGL + 4 * l); }
  asm volatile(".Lfk_end:" ::: "memory");
}

extern "C" void kernel_launch(void* const* d_in, const int* in_sizes, int n_in, void* d_out, int out_size, void* d_ws,
                              size_t ws_size, hipStream_t stream) {
  (void)hipFuncSetAttribute((const void*)fused_kernel, hipFuncAttributeMaxDynamicSharedMemorySize, LDS_BYTES);
  fused_kernel<<<256, 512, LDS_BYTES, stream>>>(d_in[0], d_in[1], d_in[2], d_in[4], d_in[5], d_in[6], (char*)d_out, (char*)d_ws);
}
```

```cpp
#define STAMPS
#define Q 0
#include <hip/hip_runtime.h>
#include <stdint.h>
typedef __bf16 bf8 __attribute__((ext_vector_type(8)));
typedef __bf16 bf2 __attribute__((ext_vector_type(2)));
typedef float f4 __attribute__((ext_vector_type(4)));
typedef float f2 __attribute__((ext_vector_type(2)));
typedef unsigned u4 __attribute__((ext_vector_type(4)));
typedef unsigned u2 __attribute__((ext_vector_type(2)));
typedef short s4 __attribute__((ext_vector_type(4)));
typedef unsigned long long u64;
#define AS3 __attribute__((address_space(3)))
typedef __amdgpu_buffer_rsrc_t rsrc_t;

constexpr int XR = 0, CR = 65536, WR = CR + 30720, GATE = 157696, MISC = 158976, KF = MISC + 64, DBGL = KF + 2048;
constexpr int SCR = CR + 5 * 10240, KVPITCH = 5184, LDS_BYTES = 163840;
constexpr int WS_F1 = 1024, WS_F2 = 2048, WS_F3B = 4096, WS_F3A = 6144, WS_DBG = 65536;
constexpr int WS_KV = 1 << 20, WS_KVP = 7 << 20, WS_OB = 8 << 20, WS_OA = 11 << 20, WS_BYTES = 16 << 20;
#define PUB 3
#define CHK 7
constexpr int SPIN_MAX = 200000;

template <int N> __device__ __forceinline__ void waitv() { asm volatile("s_waitcnt vmcnt(%0)" ::"i"(N) : "memory"); }
__device__ __forceinline__ void waitvn(int n) { asm volatile("s_waitcnt vmcnt(%0)" ::"i"(n) : "memory"); }
__device__ __forceinline__ void waitl() { asm volatile("" ::: "memory"); __builtin_amdgcn_s_waitcnt(0xC07F); asm volatile("" ::: "memory"); }
__device__ __forceinline__ void bar() { asm volatile("" ::: "memory"); __builtin_amdgcn_s_barrier(); asm volatile("" ::: "memory"); }
template <int NL, int SC1> __device__ __forceinline__ void dma(u4 rs, int lds, int voff, int soff) {
  if constexpr (NL == 64 && !SC1)
    asm volatile("s_mov_b32 m0, %0\n\ts_nop 0\n\tbuffer_load_dwordx4 %1, %2, %3 offen lds" ::"s"(lds), "v"(voff), "s"(rs), "s"(soff) : "memory");
  else if constexpr (NL == 64)
    asm volatile("s_mov_b32 m0, %0\n\ts_nop 0\n\tbuffer_load_dwordx4 %1, %2, %3 offen sc1 lds" ::"s"(lds), "v"(voff), "s"(rs), "s"(soff) : "memory");
  else if constexpr (NL >= 32)
    asm volatile("s_mov_b32 m0, %0\n\ts_mov_b32 exec_hi, %4\n\tbuffer_load_dwordx4 %1, %2, %3 offen lds\n\ts_mov_b32 exec_hi, -1" ::"s"(lds), "v"(voff), "s"(rs), "s"(soff), "i"((1 << (NL - 32)) - 1) : "memory");
  else
    asm volatile("s_mov_b32 m0, %0\n\ts_mov_b64 exec, %4\n\tbuffer_load_dwordx4 %1, %2, %3 offen lds\n\ts_mov_b64 exec, -1" ::"s"(lds), "v"(voff), "s"(rs), "s"(soff), "i"((1 << NL) - 1) : "memory");
}
__device__ __forceinline__ u4 mkrs(const void* p, unsigned bytes) {
  u64 a = (u64)p;
  return u4{(unsigned)a, (unsigned)(a >> 32) & 0xffffu, bytes, 0x00020000u};
}
__device__ __forceinline__ u4 bld128(u4 rs, int voff, int soff) {
  u4 d; asm volatile("buffer_load_dwordx4 %0, %1, %2, %3 offen sc1" : "=v"(d) : "v"(voff), "s"(rs), "s"(soff) : "memory"); return d;
}
template <int OFF> __device__ __forceinline__ u2 bld64(u4 rs, int voff, int soff) {
  u2 d; asm volatile("buffer_load_dwordx2 %0, %1, %2, %3 offen offset:%4 sc1" : "=v"(d) : "v"(voff), "s"(rs), "s"(soff), "i"(OFF) : "memory"); return d;
}
__device__ __forceinline__ void waitv_on(int n, u4& a) { asm volatile("s_waitcnt vmcnt(%1)" : "+v"(a) : "i"(n) : "memory"); }
__device__ __forceinline__ void waitv_on(int n, u2& a, u2& b) { asm volatile("s_waitcnt vmcnt(%2)" : "+v"(a), "+v"(b) : "i"(n) : "memory"); }
#define SB() __builtin_amdgcn_sched_barrier(0)
__device__ __forceinline__ u4 lr128(int a) { return *(const AS3 u4*)(size_t)a; }
__device__ __forceinline__ u2 lr64(int a) { return *(const AS3 u2*)(size_t)a; }
__device__ __forceinline__ void lw128(int a, u4 v) { *(AS3 u4*)(size_t)a = v; }
__device__ __forceinline__ void lw64(int a, u2 v) { *(AS3 u2*)(size_t)a = v; }
__device__ __forceinline__ u2 ltr(int a) {
  s4 v = __builtin_amdgcn_ds_read_tr16_b64_v4i16((AS3 s4*)(size_t)a);
  return __builtin_bit_cast(u2, v);
}
__device__ __forceinline__ u4 cat(u2 a, u2 b) { return u4{a.x, a.y, b.x, b.y}; }
__device__ __forceinline__ f4 mf(u4 a, u4 b, f4 c) {
  return __builtin_amdgcn_mfma_f32_16x16x32_bf16(__builtin_bit_cast(bf8, a), __builtin_bit_cast(bf8, b), c, 0, 0, 0);
}
__device__ __forceinline__ unsigned pk(float a, float b) {
  f2 v = {a, b};
  return __builtin_bit_cast(unsigned, __builtin_convertvector(v, bf2));
}
__device__ __forceinline__ u2 pk4(f4 v) { return u2{pk(v.x, v.y), pk(v.z, v.w)}; }
__device__ __forceinline__ float phi(float t) { return (t > 0.f ? t + 1.f : __builtin_amdgcn_exp2f(t * 1.44269504089f)) + 1e-6f; }
__device__ __forceinline__ f4 phi4(f4 v) { return f4{phi(v.x), phi(v.y), phi(v.z), phi(v.w)}; }
__device__ __forceinline__ u2 swap16(unsigned a, unsigned b) {
  auto r = __builtin_amdgcn_permlane16_swap(a, b, false, false);
  return u2{r[0], r[1]};
}
__device__ __forceinline__ u2 swap32(unsigned a, unsigned b) {
  auto r = __builtin_amdgcn_permlane32_swap(a, b, false, false);
  return u2{r[0], r[1]};
}
__device__ __forceinline__ float asf(unsigned x) { return __builtin_bit_cast(float, x); }
__device__ __forceinline__ unsigned asu(float x) { return __builtin_bit_cast(unsigned, x); }
__device__ __forceinline__ void flag_store(char* ws, int off, u64 tag) {
  __hip_atomic_store((u64*)(ws + off), tag, __ATOMIC_RELAXED, __HIP_MEMORY_SCOPE_AGENT);
}
__device__ __forceinline__ void poll(char* ws, int off, bool act, u64 tag) {
#pragma unroll 1
  for (int it = 0; it < SPIN_MAX; ++it) {
    u64 v = __hip_atomic_load((u64*)(ws + off), __ATOMIC_RELAXED, __HIP_MEMORY_SCOPE_AGENT);
    if (__builtin_amdgcn_ballot_w64(act && v != tag) == 0) break;
    __builtin_amdgcn_s_sleep(2);
  }
  asm volatile("" ::: "memory");
}
#define STAMP(i) do { if (w == STAMPW) { unsigned t_ = (unsigned)__builtin_amdgcn_s_memrealtime(); *(AS3 unsigned*)(size_t)(DBGL + 4 * (i)) = t_; } } while (0)
#define STAMPW 0

__global__ __launch_bounds__(512, 2) void fused_kernel(const void* __restrict__ px, const void* __restrict__ pwa,
                                                       const void* __restrict__ pwoa, const void* __restrict__ pwb,
                                                       const void* __restrict__ pwob, const void* __restrict__ pgw,
                                                       char* __restrict__ out, char* __restrict__ ws) {
  const int tid = threadIdx.x, l = tid & 63, w = __builtin_amdgcn_readfirstlane(tid >> 6);
  const int bid = blockIdx.x, xcd = bid & 7, idx = bid >> 3;
  {
    u64 pc; unsigned sz;
    asm volatile("s_getpc_b64 %0\n.Lfk_start:\n s_mov_b32 %1, .Lfk_end-.Lfk_start" : "=s"(pc), "=s"(sz));
    unsigned o = (unsigned)((((2 * idx + w) & 7) * 64 + l) * 128);
    if (w < 2 && o < sz)
      asm volatile("s_mov_b32 m0, %2\n\ts_nop 0\n\tglobal_load_lds_dword %0, %1" ::"v"(o), "s"(pc), "s"(DBGL + 256) : "memory");
  }
  const int r = l & 15, g = l >> 4, q = r >> 2, p = r & 3, mw = w & 3, nw = w >> 2;
  const int cq = xcd >> 1, tt = 8 * (xcd & 1) + (idx & 7), cs = idx >> 3;
  const bool isB = cs >= 2;
  const int pair = 4 * cq + 2 * (cs & 1) + ((cs < 2 ? 1 : 0) ^ (cq & 1));
  const int rot = (idx * 3 + (idx >> 3)) % 10;
  const u4 rX = mkrs(px, 2048 * 1280), rWA = mkrs(pwa, 640 * 3840), rWB = mkrs(pwb, 640 * 3840);
  const u4 rW1 = mkrs(isB ? pwb : pwa, 640 * 3840), rWSd = mkrs(ws, WS_BYTES);
  const rsrc_t rWS = __builtin_amdgcn_make_buffer_rsrc((void*)ws, 0, WS_BYTES, 0x00020000);
  STAMP(0);
  int xv0, xv1, wv[3];
  {
    int row = 16 * w + (l >> 3);
    xv0 = row * 1280 + (((l & 7) ^ ((row >> 1) & 7)) << 4);
    xv1 = (xv0 ^ 64) + 8 * 1280;
    const int base0 = isB ? 1280 + 160 * (2 * cq + cs - 2) : 1280 + 1280 * cs + 320 * cq;
    const int delta = isB ? 1120 : 0;
#pragma unroll
    for (int i = 0; i < 3; ++i) {
      int ci = 64 * i + l, rw = ci / 20, pos = ci - rw * 20;
      int t = pos ^ (2 * (w & 1));
      int c = t >= 10 ? t ^ 1 : t;
      wv[i] = (8 * w + rw) * 3840 + base0 + 16 * c + (c >= 10 ? delta : 0);
    }
  }
  const int xs0 = tt * (128 * 1280);
  auto kchunk = [&](int s) { int k = s + rot; return k >= 10 ? k - 10 : k; };
  auto issx = [&](int s, int i) { dma<64, 0>(rX, XR + (s & 3) * 16384 + w * 2048 + i * 1024, i ? xv1 : xv0, xs0 + kchunk(s) * 128); };
  auto issw = [&](int s, int i) {
    int wd = WR + ((s + 2) % 3) * 20480 + w * 2560 + i * 1024, so = kchunk(s) * 245760;
    if (i < 2) dma<64, 0>(rW1, wd, wv[i], so); else dma<32, 0>(rW1, wd, wv[2], so);
  };
#pragma unroll
  for (int s = 0; s < 3; ++s) { issx(s, 0); issx(s, 1); issw(s, 0); issw(s, 1); issw(s, 2); }
  SB();
  u64 tag;
  asm volatile("s_load_dwordx2 %0, %1, 0x0" : "=s"(tag) : "s"(ws) : "memory");
  int qva, qvb;
  {
    int pr = l / 5, c = l - pr * 5;
    int kb = 8 * w + (pr >> 1);
    qva = (kb + 4 * (pr & 1)) * 3840 + 80 * pair + 16 * c;
    qvb = (kb + 4 * ((pr & 1) ^ 1)) * 3840 + 80 * pair + 16 * c;
  }
  auto iss2 = [&](int rs, int i) {
    dma<40, 0>(i ? rWB : rWA, CR + (rs % 9) * 10240 + w * 640 + i * 5120, i ? qvb : qva, kchunk((rs + 6) % 10) * 245760);
  };
  auto issue2 = [&](int rs) { iss2(rs, 0); iss2(rs, 1); };
  int wa[5];
#pragma unroll
  for (int nt = 0; nt < 5; ++nt) {
    int byteA = 160 * nw + 32 * nt + 8 * p;
    int byteB = nt < 4 ? 160 * (nt >> 1) + 80 * nw + 40 * (nt & 1) + 8 * p : 160 * (p >> 1) + 80 * nw + 40 * (p & 1) + 32;
    int by = isB ? byteB : byteA;
    if (by >= 160) by ^= 16;
    by ^= (g & 1) << 5;
    wa[nt] = WR + (8 * g + q) * 320 + by;
  }
  const int xa0 = XR + (32 * mw + r) * 128 + ((g ^ (r >> 1)) << 4), xa1 = xa0 ^ 64;
  if (tid < 4) *(AS3 unsigned*)(size_t)(MISC + 4 * tid) = 0;

  u4 xk[10][2][2];
  u4 wf[2][5];
  f4 acc[2][5];
#pragma unroll
  for (int i = 0; i < 5; ++i) acc[0][i] = acc[1][i] = f4{0, 0, 0, 0};
  auto rdx = [&](int s, int kk) {
    int xo = (s & 3) * 16384;
    xk[s][kk][0] = lr128((kk ? xa1 : xa0) + xo);
    xk[s][kk][1] = lr128((kk ? xa1 : xa0) + xo + 2048);
  };
  auto rdw = [&](int s, int kk, int nt) {
    int wo = ((s + 2) % 3) * 20480 + kk * 10240;
    wf[kk][nt] = cat(ltr(wa[nt] + wo), ltr(wa[nt] + wo + 1280));
  };
  auto mm1 = [&](int s, int kk, int nt) {
    acc[0][nt] = mf(xk[s][kk][0], wf[kk][nt], acc[0][nt]);
    acc[1][nt] = mf(xk[s][kk][1], wf[kk][nt], acc[1][nt]);
  };
  int gv;
  {
    int ci = 10 * w + l, kc = (ci >> 3) + 6 + rot;
    kc = kc >= 20 ? kc - 20 : (kc >= 10 ? kc - 10 : kc);
    gv = kc * 128 + (ci & 7) * 16;
  }
  SB();
  asm volatile("s_waitcnt vmcnt(10) lgkmcnt(0)" : "+s"(tag)::"memory");
  tag += 1;
  bar();
  STAMP(1);
  rdx(0, 0);
#pragma unroll
  for (int nt = 0; nt < 5; ++nt) rdw(0, 0, nt);
  SB();
#pragma unroll
  for (int s = 0; s < 10; ++s) {
    rdx(s, 1); rdw(s, 1, 0); mm1(s, 0, 0); SB();
    rdw(s, 1, 1); rdw(s, 1, 2); mm1(s, 0, 1); SB();
    rdw(s, 1, 3); rdw(s, 1, 4); mm1(s, 0, 2); SB();
    if (s <= 6) issx(s + 3, 0);
    if (s == 7) dma<10, 0>(mkrs(pgw, 1280), GATE + 160 * w, gv, 0);
    if (s == 8) iss2(1, 1);
    if (s == 9) iss2(3, 1);
    mm1(s, 0, 3); SB();
    if (s <= 6) issx(s + 3, 1);
    if (s == 7) iss2(0, 0);
    if (s == 8) iss2(2, 0);
    if (s == 9) iss2(4, 0);
    mm1(s, 0, 4); SB();
    waitl();
    if (s < 8) waitv<7>();
    if (s == 8) waitv<6>();
    bar(); SB();
    if (s < 9) { rdx(s + 1, 0); rdw(s + 1, 0, 0); }
    mm1(s, 1, 0); SB();
    if (s < 9) { rdw(s + 1, 0, 1); rdw(s + 1, 0, 2); }
    mm1(s, 1, 1); SB();
    if (s < 9) { rdw(s + 1, 0, 3); rdw(s + 1, 0, 4); }
    if (s <= 6) issw(s + 3, 0);
    mm1(s, 1, 2); SB();
    if (s <= 6) issw(s + 3, 1);
    if (s == 7) iss2(0, 1);
    if (s == 8) iss2(2, 1);
    if (s == 9) iss2(4, 1);
    mm1(s, 1, 3); SB();
    if (s <= 6) issw(s + 3, 2);
    if (s == 7) iss2(1, 0);
    if (s == 8) iss2(3, 0);
    mm1(s, 1, 4); SB();
  }
  STAMP(2);
  if (!isB) {
    const bool isV = cs == 1;
#pragma unroll
    for (int nt = 0; nt < 5; ++nt) {
      int col = 80 * nw + 16 * nt + r, hl = col / 20, d = col - hl * 20;
      u2 lo = pk4(acc[0][nt]), hi = pk4(acc[1][nt]);
      bool sw = !isV && ((d >> 2) & 1);
      u4 v = sw ? cat(hi, lo) : cat(lo, hi);
      int ch = isV ? g ^ (((d >> 3) & 1) << 1) : g;
      int off = (hl >> 1) * (64 * 5120) + (hl & 1) * 1280 + d * 64 + ch * 16;
      __builtin_amdgcn_raw_buffer_store_b128(v, rWS, off + WS_KV + 4 * cq * (64 * 5120) + (4 * tt + mw) * 5120 + (isV ? 2560 : 0), 0, 16);
    }
    issue2(5); issue2(6); issue2(7); issue2(8);
    waitv<8 + 5 + 8>();
  } else {
    u4 k0 = cat(pk4(phi4(acc[0][0])), pk4(phi4(acc[1][0])));
    u4 k1 = cat(pk4(phi4(acc[0][1])), pk4(phi4(acc[1][1])));
    u4 kr = cat(pk4(phi4(acc[0][4])), pk4(phi4(acc[1][4])));
    u4 v0 = cat(pk4(acc[0][2]), pk4(acc[1][2]));
    u4 v1 = cat(pk4(acc[0][3]), pk4(acc[1][3]));
    u4 vr = cat(pk4(acc[0][4]), pk4(acc[1][4]));
    const unsigned one2 = 0x3F803F80u;
    if (r < 8) vr = u4{one2, one2, one2, one2};
    u4 kh;
#pragma unroll
    for (int i = 0; i < 4; ++i) kh[i] = __builtin_amdgcn_update_dpp(0u, kr[i], 0x118, 0xf, 0xf, true);
    const f4 z = {0, 0, 0, 0};
    f4 n0 = mf(k0, v0, z), n1 = mf(k1, v1, z), m2 = mf(k0, vr, z), m3 = mf(k1, vr, z);
    f4 m4 = mf(kr, v0, z), m5 = mf(kr, v1, z), m6 = mf(kh, vr, z);
    f4 n2 = (q & 1) ? m3 : m2;
    f4 n3 = g == 0 ? m4 : (g == 1 ? m5 : m6);
    int sa = SCR + w * 4096 + l * 16;
    lw128(sa, __builtin_bit_cast(u4, n0)); lw128(sa + 1024, __builtin_bit_cast(u4, n1));
    lw128(sa + 2048, __builtin_bit_cast(u4, n2)); lw128(sa + 3072, __builtin_bit_cast(u4, n3));
    waitl(); bar();
    int ra = SCR + nw * 16384 + mw * 1024 + l * 16;
    f4 s0 = __builtin_bit_cast(f4, lr128(ra)), s1 = __builtin_bit_cast(f4, lr128(ra + 4096));
    f4 s2 = __builtin_bit_cast(f4, lr128(ra + 8192)), s3 = __builtin_bit_cast(f4, lr128(ra + 12288));
    f4 sm = (s0 + s1) + (s2 + s3);
    int pb = 2 * (2 * cq + cs - 2) + nw;
    __builtin_amdgcn_raw_buffer_store_b128(__builtin_bit_cast(u4, sm), rWS, (l >> 5) * 8192 + (l & 31) * 16 +
                                           WS_KVP + pb * 65536 + mw * 16384 + tt * 512, 0, 16);
    bar();
    issue2(5); issue2(6); issue2(7); issue2(8);
    waitv<8 + 1 + 8>();
  }
  bar();
  STAMP(3);
  f4 qa[2], qb[2], qr[2];
  qa[0] = qa[1] = qb[0] = qb[1] = qr[0] = qr[1] = f4{0, 0, 0, 0};
  u4 gth[8];
  {
    const int a0 = CR + (8 * g + 2 * q) * 80 + 40 * nw + 8 * p;
    const int rb = CR + (8 * g + 2 * q) * 80 + 40 * nw + 32;
    const int r0 = rb + ((p & 1) ? 5120 + 80 : 0), r1 = rb + ((p & 1) ? 5120 : 80);
    const int ga = GATE + g * 16;
    u4 fa[2], fb[2], fr[2], fg[2], xq[2][2];
    auto rd2 = [&](int rs, int kk) {
      int o = (rs % 9) * 10240 + kk * 2560;
      fa[kk] = cat(ltr(a0 + o), ltr(a0 + o + 80));
      fb[kk] = cat(ltr(a0 + o + 5120 + 80), ltr(a0 + o + 5120));
      fr[kk] = cat(ltr(r0 + o), ltr(r1 + o));
      fg[kk] = lr128(ga + rs * 128 + kk * 64);
      if (rs < 4) {
        int xo = ((rs + 6) & 3) * 16384;
        xq[kk][0] = lr128((kk ? xa1 : xa0) + xo);
        xq[kk][1] = lr128((kk ? xa1 : xa0) + xo + 2048);
      }
    };
    auto mm2 = [&](int rs, int kk) {
      u4 x0 = rs < 4 ? xq[kk][0] : xk[rs >= 4 ? rs - 4 : 0][kk][0];
      u4 x1 = rs < 4 ? xq[kk][1] : xk[rs >= 4 ? rs - 4 : 0][kk][1];
      u4 f = r >= 8 ? fg[kk] : fr[kk];
      qa[0] = mf(fa[kk], x0, qa[0]); qa[1] = mf(fa[kk], x1, qa[1]);
      qb[0] = mf(fb[kk], x0, qb[0]); qb[1] = mf(fb[kk], x1, qb[1]);
      qr[0] = mf(f, x0, qr[0]); qr[1] = mf(f, x1, qr[1]);
    };
    rd2(0, 0);
#pragma unroll
    for (int rs = 0; rs < 10; ++rs) {
      rd2(rs, 1);
      mm2(rs, 0);
      waitl();
      if (rs <= CHK) {
        if (rs == CHK) waitvn(0);
        else if (rs == 0) waitvn(PUB == 0 ? 8 : 15);
        else if (rs < 4) waitvn(rs < PUB ? 2 * (3 - rs) + 11 : 10);
        else waitvn(rs < 8 ? 2 * (8 - rs) : 0);
        if (rs == CHK) {
          if (w == 0) {
            int t2 = tt - 2 + (l & 3);
            bool a1 = l < 8 && (l & 3) < 3 && t2 >= 0;
            int off = l < 8 ? WS_F1 + ((l >> 2) * 64 + cq * 16 + (t2 < 0 ? 0 : t2)) * 8 : WS_F2 + ((pair >> 1) * 16 + (l & 15)) * 8;
            poll(ws, off, a1 || (l >= 16 && l < 32), tag);
          }
        }
        bar();
      }
      if (rs == 0) issue2(9);
      if (rs == PUB && tid == 0) flag_store(ws, isB ? WS_F2 + ((2 * cq + cs - 2) * 16 + tt) * 8 : WS_F1 + (cs * 64 + cq * 16 + tt) * 8, tag);
      if (rs == CHK) {
        STAMP(4);
#pragma unroll
        for (int i = 0; i < 8; ++i) {
          int n = 8 * i + w;
          if (n < 60) {
            int b = n / 5, pc = n - 5 * b, gb = 4 * (tt - 2) + b;
            gb = gb < 0 ? 4 * tt : gb;
            dma<64, 1>(rWSd, b * KVPITCH + pc * 1024, l * 16, WS_KV + pair * (64 * 5120) + gb * 5120 + pc * 1024);
          }
        }
#pragma unroll
        for (int i = 0; i < 8; ++i)
          gth[i] = __builtin_amdgcn_raw_buffer_load_b128(rWS, l * 16, WS_KVP + pair * 65536 + w * 8192 + i * 1024, 16);
        if (tid < 24) {
          int b = tid >> 1;
          unsigned v = (tid & 1) ? 0x3F803F80u : (4 * (tt - 2) + b < 0 ? 0xF149F149u : 0u);
          lw128(b * KVPITCH + 5120 + (tid & 1) * 16, u4{v, v, v, v});
        }
      }
      if (rs < 9) rd2(rs + 1, 0);
      mm2(rs, 1);
    }
  }
  STAMP(5);
  {
    f4 s = __builtin_bit_cast(f4, gth[0]);
#pragma unroll
    for (int i = 1; i < 8; ++i) s += __builtin_bit_cast(f4, gth[i]);
    f4 o;
#pragma unroll
    for (int i = 0; i < 4; ++i) { u2 t = swap32(asu(s[i]), asu(s[i])); o[i] = asf(t.x) + asf(t.y); }
    if (l < 32) lw64(KF + w * 256 + l * 8, pk4(o));
  }
  float gate[2];
  u4 qf[2], pf[2];
#pragma unroll
  for (int mt = 0; mt < 2; ++mt) {
    float lg = asf(swap32(asu(qr[mt][0]), asu(qr[mt][0])).y);
    gate[mt] = __builtin_amdgcn_rcpf(1.f + __builtin_amdgcn_exp2f(-1.44269504089f * lg));
    u2 rr = pk4(qr[mt]);
    u2 up = g == 0 ? rr : (g == 1 ? u2{0x3F80u, 0u} : u2{0u, 0u});
    qf[mt] = cat(pk4(qa[mt]), up);
    u2 pr = pk4(phi4(qr[mt]));
    pf[mt] = cat(pk4(phi4(qb[mt])), g == 1 ? pr : u2{0u, 0u});
  }
  waitv<0>(); waitl(); bar();
  STAMP(6);
  const int ov = (g & 1) * 20480 + (2 * nw + (g >> 1)) * 256 + r * 16;
  const int ovr = (g & 1) * 20480 + 16384 + (pair >> 2) * 1024 + (pair & 3) * 256 + nw * 128 + r * 8;
  const int os = (8 * tt + 2 * mw) * 20480;
  auto store_o = [&](f4 m0, f4 m1, f4 e0, f4 e1, int base) {
    u2 a = pk4(m0), b = pk4(m1);
    u2 t0 = swap16(a.x, b.x), t1 = swap16(a.y, b.y);
    __builtin_amdgcn_raw_buffer_store_b128(u4{t0.x, t1.x, t0.y, t1.y}, rWS, ov + base + os + pair * 1024, 0, 16);
    u2 c = pk4(e0), d = pk4(e1);
    u2 y = u2{swap16(c.x, d.x).x, swap16(c.y, d.y).x};
    if (l < 32) __builtin_amdgcn_raw_buffer_store_b64(y, rWS, ovr, base + os, 16);
  };
  {
    int rp = r < 4 ? 8 + 4 * nw + r : 4 * nw;
    u4 a0 = cat(lr64(KF + nw * 512 + l * 8), lr64(KF + 1536 + (16 * nw + r) * 8));
    u4 a1 = cat(lr64(KF + 1024 + (16 * g + rp) * 8), lr64(KF + 1536 + (32 + 16 * nw + rp) * 8));
    const f4 z = {0, 0, 0, 0};
    f4 m[2], e[2];
#pragma unroll
    for (int mt = 0; mt < 2; ++mt) {
      m[mt] = mf(a0, pf[mt], z);
      e[mt] = mf(a1, pf[mt], z);
      float den = asf(swap16(asu(e[mt][0]), asu(e[mt][0])).y) + 1e-6f;
      float sc = (1.f - gate[mt]) * __builtin_amdgcn_rcpf(den);
      m[mt] *= sc; e[mt] *= sc;
    }
    store_o(m[0], m[1], e[0], e[1], WS_OB);
  }
  const int u = 4 * xcd + (idx >> 3), cb = idx & 7;
  const u4 rOA = mkrs(pwoa, 640 * 1280), rOB = mkrs(pwob, 640 * 1280);
  int ovm, ove;
  {
    int pr = l / 10, c = l - 10 * pr;
    int rho = 16 * (w >> 2) + 8 * (w & 1) + 4 * ((w >> 1) & 1) + pr;
    ovm = (20 * (rho >> 4) + (rho & 15)) * 1280 + 160 * cb + 16 * c;
    ove = (20 * (rho >> 2) + 16 + (rho & 3)) * 1280 + 160 * cb + 16 * c;
  }
  auto issue3 = [&](u4 rs, int kh, int i, int slot) {
    if (i < 8) dma<40, 0>(rs, slot * 5120 + w * 640, ovm, (8 * kh + i) * 51200);
    else dma<40, 0>(rs, slot * 5120 + w * 640, ove, (2 * kh + i - 8) * 204800);
  };
  {
#pragma unroll
    for (int i = 0; i < 9; ++i) { issue3(rOB, 0, i, 13 + i); issue3(rOB, 1, i, 22 + i); }
  }
  STAMP(7);
  {
    const int kb = mw * KVPITCH + nw * 1280;
    const int ka0 = kb + (4 * g + q) * 64 + p * 16 + 8 * (g & 1), ka1 = ka0 ^ 8;
    const int kr0 = (g & 1) ? mw * KVPITCH + 5120 + 8 : kb + (16 + q) * 64 + p * 16, kr1 = kr0 ^ 8;
    f4 S[2][18];
    const f4 z = {0, 0, 0, 0};
#pragma unroll
    for (int t = 0; t < 18; ++t) {
      int o = (t >> 1) * KVPITCH;
      u4 kf = cat(ltr(((t & 1) ? ka1 : ka0) + o), ltr(((t & 1) ? kr1 : kr0) + o));
      if (t < 17) S[0][t] = mf(kf, qf[0], z);
      if (t > 0) S[1][t] = mf(kf, qf[1], z);
    }
    waitv<18>();
    if (l == 0) {
      unsigned c = __hip_atomic_fetch_add((AS3 unsigned*)(size_t)MISC, 1u, __ATOMIC_RELAXED, __HIP_MEMORY_SCOPE_WORKGROUP);
      if (c == 7) flag_store(ws, WS_F3B + (tt * 16 + pair) * 8, tag);
    }
    const float C = 0.22360679775f * 1.44269504089f;
    u2 P[2][18];
    unsigned mk0 = (4 * g <= r ? 0xFFFFu : 0u) | (4 * g + 1 <= r ? 0xFFFF0000u : 0u);
    unsigned mk1 = (4 * g + 2 <= r ? 0xFFFFu : 0u) | (4 * g + 3 <= r ? 0xFFFF0000u : 0u);
#pragma unroll
    for (int mt = 0; mt < 2; ++mt) {
      f4 sm;
#pragma unroll
      for (int i = 0; i < 4; ++i) sm[i] = (4 * g + i <= r) ? S[mt][16 + mt][i] : S[mt][mt][i];
      S[mt][mt] = sm;
      float mx = fmaxf(fmaxf(sm[0], sm[1]), fmaxf(sm[2], sm[3]));
#pragma unroll
      for (int t = 1; t < 16; ++t) {
        f4 s = S[mt][mt + t];
        mx = fmaxf(fmaxf(mx, s[0]), s[1]);
        mx = fmaxf(fmaxf(mx, s[2]), s[3]);
      }
      u2 t16 = swap16(asu(mx), asu(mx));
      mx = fmaxf(asf(t16.x), asf(t16.y));
      u2 t32 = swap32(asu(mx), asu(mx));
      mx = fmaxf(asf(t32.x), asf(t32.y));
      const float nm = -mx * C;
#pragma unroll
      for (int t = 0; t < 16; ++t) {
        f4 s = S[mt][mt + t] * C + nm;
        f4 e = {__builtin_amdgcn_exp2f(s[0]), __builtin_amdgcn_exp2f(s[1]), __builtin_amdgcn_exp2f(s[2]), __builtin_amdgcn_exp2f(s[3])};
        P[mt][mt + t] = pk4(e);
      }
      u2 pm = P[mt][mt];
      P[mt][16 + mt] = u2{pm.x & mk0, pm.y & mk1};
      P[mt][mt] = u2{pm.x & ~mk0, pm.y & ~mk1};
      P[mt][17 - 17 * mt] = u2{0u, 0u};
    }
    const int vb = mw * KVPITCH + (2 + nw) * 1280;
    const int va0 = vb + r * 64 + ((g ^ (((r >> 3) & 1) << 1)) << 4);
    const int va1 = r < 4 ? vb + (16 + r) * 64 + g * 16 : mw * KVPITCH + 5136;
    f4 m[2] = {z, z}, e[2] = {z, z};
#pragma unroll
    for (int i = 0; i < 9; ++i) {
      u4 v0 = lr128(va0 + i * KVPITCH), v1 = lr128(va1 + i * KVPITCH);
#pragma unroll
      for (int mt = 0; mt < 2; ++mt) {
        u4 pp = cat(P[mt][2 * i], P[mt][2 * i + 1]);
        m[mt] = mf(v0, pp, m[mt]);
        e[mt] = mf(v1, pp, e[mt]);
      }
    }
#pragma unroll
    for (int mt = 0; mt < 2; ++mt) {
      float den = asf(swap16(asu(e[mt][0]), asu(e[mt][0])).y);
      float sc = gate[mt] * __builtin_amdgcn_rcpf(den);
      m[mt] *= sc; e[mt] *= sc;
    }
    store_o(m[0], m[1], e[0], e[1], WS_OA);
  }
  STAMP(8);
  waitv<0>();
  if (l == 0) {
    unsigned c = __hip_atomic_fetch_add((AS3 unsigned*)(size_t)(MISC + 4), 1u, __ATOMIC_RELAXED, __HIP_MEMORY_SCOPE_WORKGROUP);
    if (c == 7) flag_store(ws, WS_F3A + (tt * 16 + pair) * 8, tag);
  }
  if (w == 0) poll(ws, WS_F3B + ((u >> 1) * 16 + (l & 15)) * 8, true, tag);
  waitl(); bar();
  STAMP(9);
  {
    const int pa = (16 * (g >> 1) + 4 * (g & 1) + q) * 160 + 8 * p;
    const int pa13 = pa + (13 + 9 * nw) * 5120;
    const int ob = (4 * u + mw) * 20480;
    f4 oc[5];
#pragma unroll
    for (int i = 0; i < 5; ++i) oc[i] = f4{0, 0, 0, 0};
    u4 of[8];
    u2 orl[2], orh[2];
    auto ldo = [&](int base) {
#pragma unroll
      for (int i = 0; i < 8; ++i) of[i] = bld128(rWSd, l * 16, base + ob + (8 * nw + i) * 1024);
#pragma unroll
      for (int i = 0; i < 2; ++i) {
        int so = base + ob + 16384 + (2 * nw + i) * 1024;
        orl[i] = bld64<0>(rWSd, g * 256 + r * 8, so);
        orh[i] = bld64<128>(rWSd, g * 256 + r * 8, so);
      }
    };
    auto step = [&](int a, int i, int later) {
      u4 wfr[5];
#pragma unroll
      for (int nt = 0; nt < 5; ++nt) wfr[nt] = cat(ltr(a + 32 * nt), ltr(a + 32 * nt + 1280));
      if (i < 8) waitv_on(11 - i + later, of[i]); else waitv_on(2 * (9 - i) + later, orl[i - 8], orh[i - 8]);
      u4 o = i < 8 ? of[i] : cat(orl[i - 8], orh[i - 8]);
#pragma unroll
      for (int nt = 0; nt < 5; ++nt) oc[nt] = mf(wfr[nt], o, oc[nt]);
    };
    ldo(WS_OB);
    issue3(rOB, 0, 9, 0); issue3(rOB, 1, 9, 1);
#pragma unroll
    for (int i = 0; i < 5; ++i) { issue3(rOA, 0, i, 2 + i); issue3(rOA, 1, i, 7 + i); }
#pragma unroll
    for (int i = 0; i < 9; ++i) step(pa13 + i * 5120, i, 12);
    waitl(); waitv<10>(); bar();
    STAMP(10);
#pragma unroll
    for (int i = 5; i < 10; ++i) { issue3(rOA, 0, i, 13 + i - 5); issue3(rOA, 1, i, 22 + i - 5); }
    step(pa + nw * 5120, 9, 20);
    SB();
    waitv<0>();
    if (w == 0) poll(ws, WS_F3A + ((u >> 1) * 16 + (l & 15)) * 8, true, tag);
    bar();
    STAMP(11);
    ldo(WS_OA);
#pragma unroll
    for (int i = 0; i < 5; ++i) step(pa + (2 + 5 * nw + i) * 5120, i, 0);
#pragma unroll
    for (int i = 5; i < 10; ++i) step(pa13 + (i - 5) * 5120, i, 0);
    STAMP(12);
    const int xa = 18 * 5120 + mw * 5120 + l * 16;
    if (nw) { lw128(xa, __builtin_bit_cast(u4, oc[0])); lw128(xa + 1024, __builtin_bit_cast(u4, oc[1])); }
    else { lw128(xa + 2048, __builtin_bit_cast(u4, oc[2])); lw128(xa + 3072, __builtin_bit_cast(u4, oc[3])); lw128(xa + 4096, __builtin_bit_cast(u4, oc[4])); }
    waitl(); bar();
    char* op = out + (size_t)((64 * u + 16 * mw + r) * 1280 + 160 * cb + 8 * g);
    if (nw) {
#pragma unroll
      for (int nt = 2; nt < 5; ++nt) *(u2*)(op + 32 * nt) = pk4(oc[nt] + __builtin_bit_cast(f4, lr128(xa + 1024 * nt)));
    } else {
#pragma unroll
      for (int nt = 0; nt < 2; ++nt) *(u2*)(op + 32 * nt) = pk4(oc[nt] + __builtin_bit_cast(f4, lr128(xa + 1024 * nt)));
    }
  }
  STAMP(13);
  if (bid == 0 && w == 0) {
#pragma unroll 1
    for (int i = 0; i < 4; ++i) poll(ws, WS_F3B + (i * 64 + l) * 8, true, tag);
    if (l == 0) flag_store(ws, 0, tag);
  }
  if (w == STAMPW) { waitl(); if (l < 16) *(unsigned*)(ws + WS_DBG + bid * 64 + l * 4) = *(AS3 unsigned*)(size_t)(DBGL + 4 * l); }
  asm volatile(".Lfk_end:" ::: "memory");
}

__global__ void k0(char* ws) { if (threadIdx.x == 0) *(unsigned*)(ws + WS_DBG + 32768) = (unsigned)__builtin_amdgcn_s_memrealtime(); }
__global__ void k2(char* ws) {
  unsigned now = (unsigned)__builtin_amdgcn_s_memrealtime();
  unsigned tk0 = *(unsigned*)(ws + WS_DBG + 32768);
  unsigned mn0 = ~0u, mx0 = 0, mx13 = 0;
  for (int b = 0; b < 256; ++b) {
    unsigned a = *(unsigned*)(ws + WS_DBG + b * 64) - tk0, c = *(unsigned*)(ws + WS_DBG + b * 64 + 52) - tk0;
    mn0 = a < mn0 ? a : mn0; mx0 = a > mx0 ? a : mx0; mx13 = c > mx13 ? c : mx13;
  }
  unsigned d = Q == 0 ? 0 : Q == 1 ? mn0 : Q == 2 ? mx0 - mn0 : Q == 3 ? mx13 - mn0 : (now - tk0) - mx13;
  d = __builtin_amdgcn_readfirstlane(d);
  if (d > 10000u) d = 10000u;
  unsigned t0 = (unsigned)__builtin_amdgcn_s_memrealtime();
#pragma unroll 1
  while ((unsigned)__builtin_amdgcn_s_memrealtime() - t0 < d * 16u) __builtin_amdgcn_s_sleep(8);
}
extern "C" void kernel_launch(void* const* d_in, const int* in_sizes, int n_in, void* d_out, int out_size, void* d_ws,
                              size_t ws_size, hipStream_t stream) {
  (void)hipFuncSetAttribute((const void*)fused_kernel, hipFuncAttributeMaxDynamicSharedMemorySize, LDS_BYTES);
  k0<<<1, 64, 0, stream>>>((char*)d_ws);
  fused_kernel<<<256, 512, LDS_BYTES, stream>>>(d_in[0], d_in[1], d_in[2], d_in[4], d_in[5], d_in[6], (char*)d_out, (char*)d_ws);
  k2<<<1, 64, 0, stream>>>((char*)d_ws);
}
```

```cpp
#include <hip/hip_runtime.h>
#include <stdint.h>
typedef __bf16 bf8 __attribute__((ext_vector_type(8)));
typedef __bf16 bf2 __attribute__((ext_vector_type(2)));
typedef float f4 __attribute__((ext_vector_type(4)));
typedef float f2 __attribute__((ext_vector_type(2)));
typedef unsigned u4 __attribute__((ext_vector_type(4)));
typedef unsigned u2 __attribute__((ext_vector_type(2)));
typedef short s4 __attribute__((ext_vector_type(4)));
typedef unsigned long long u64;
#define AS3 __attribute__((address_space(3)))
typedef __amdgpu_buffer_rsrc_t rsrc_t;

constexpr int XR = 0, CR = 65536, WR = CR + 30720, GATE = 157696, MISC = 158976, KF = MISC + 64, DBGL = KF + 2048;
constexpr int SCR = CR + 5 * 10240, KVPITCH = 5184, LDS_BYTES = 163840;
constexpr int WS_F1 = 1024, WS_F2 = 2048, WS_F3B = 4096, WS_F3A = 6144, WS_DBG = 65536;
constexpr int WS_KV = 1 << 20, WS_KVP = 7 << 20, WS_OB = 8 << 20, WS_OA = 11 << 20, WS_BYTES = 16 << 20;
#define PUB 3
#define CHK 7
constexpr int SPIN_MAX = 200000;

template <int N> __device__ __forceinline__ void waitv() { asm volatile("s_waitcnt vmcnt(%0)" ::"i"(N) : "memory"); }
__device__ __forceinline__ void waitvn(int n) { asm volatile("s_waitcnt vmcnt(%0)" ::"i"(n) : "memory"); }
__device__ __forceinline__ void waitl() { asm volatile("" ::: "memory"); __builtin_amdgcn_s_waitcnt(0xC07F); asm volatile("" ::: "memory"); }
__device__ __forceinline__ void bar() { asm volatile("" ::: "memory"); __builtin_amdgcn_s_barrier(); asm volatile("" ::: "memory"); }
template <int NL, int SC1> __device__ __forceinline__ void dma(u4 rs, int lds, int voff, int soff) {
  if constexpr (NL == 64 && !SC1)
    asm volatile("s_mov_b32 m0, %0\n\ts_nop 0\n\tbuffer_load_dwordx4 %1, %2, %3 offen lds" ::"s"(lds), "v"(voff), "s"(rs), "s"(soff) : "memory");
  else if constexpr (NL == 64)
    asm volatile("s_mov_b32 m0, %0\n\ts_nop 0\n\tbuffer_load_dwordx4 %1, %2, %3 offen sc1 lds" ::"s"(lds), "v"(voff), "s"(rs), "s"(soff) : "memory");
  else if constexpr (NL >= 32)
    asm volatile("s_mov_b32 m0, %0\n\ts_mov_b32 exec_hi, %4\n\tbuffer_load_dwordx4 %1, %2, %3 offen lds\n\ts_mov_b32 exec_hi, -1" ::"s"(lds), "v"(voff), "s"(rs), "s"(soff), "i"((1 << (NL - 32)) - 1) : "memory");
  else
    asm volatile("s_mov_b32 m0, %0\n\ts_mov_b64 exec, %4\n\tbuffer_load_dwordx4 %1, %2, %3 offen lds\n\ts_mov_b64 exec, -1" ::"s"(lds), "v"(voff), "s"(rs), "s"(soff), "i"((1 << NL) - 1) : "memory");
}
__device__ __forceinline__ u4 mkrs(const void* p, unsigned bytes) {
  u64 a = (u64)p;
  return u4{(unsigned)a, (unsigned)(a >> 32) & 0xffffu, bytes, 0x00020000u};
}
__device__ __forceinline__ u4 bld128(u4 rs, int voff, int soff) {
  u4 d; asm volatile("buffer_load_dwordx4 %0, %1, %2, %3 offen sc1" : "=v"(d) : "v"(voff), "s"(rs), "s"(soff) : "memory"); return d;
}
template <int OFF> __device__ __forceinline__ u2 bld64(u4 rs, int voff, int soff) {
  u2 d; asm volatile("buffer_load_dwordx2 %0, %1, %2, %3 offen offset:%4 sc1" : "=v"(d) : "v"(voff), "s"(rs), "s"(soff), "i"(OFF) : "memory"); return d;
}
__device__ __forceinline__ void waitv_on(int n, u4& a) { asm volatile("s_waitcnt vmcnt(%1)" : "+v"(a) : "i"(n) : "memory"); }
__device__ __forceinline__ void waitv_on(int n, u2& a, u2& b) { asm volatile("s_waitcnt vmcnt(%2)" : "+v"(a), "+v"(b) : "i"(n) : "memory"); }
#define SB() __builtin_amdgcn_sched_barrier(0)
__device__ __forceinline__ u4 lr128(int a) { return *(const AS3 u4*)(size_t)a; }
__device__ __forceinline__ u2 lr64(int a) { return *(const AS3 u2*)(size_t)a; }
__device__ __forceinline__ void lw128(int a, u4 v) { *(AS3 u4*)(size_t)a = v; }
__device__ __forceinline__ void lw64(int a, u2 v) { *(AS3 u2*)(size_t)a = v; }
__device__ __forceinline__ u2 ltr(int a) {
  s4 v = __builtin_amdgcn_ds_read_tr16_b64_v4i16((AS3 s4*)(size_t)a);
  return __builtin_bit_cast(u2, v);
}
__device__ __forceinline__ u4 cat(u2 a, u2 b) { return u4{a.x, a.y, b.x, b.y}; }
__device__ __forceinline__ f4 mf(u4 a, u4 b, f4 c) {
  return __builtin_amdgcn_mfma_f32_16x16x32_bf16(__builtin_bit_cast(bf8, a), __builtin_bit_cast(bf8, b), c, 0, 0, 0);
}
__device__ __forceinline__ unsigned pk(float a, float b) {
  f2 v = {a, b};
  return __builtin_bit_cast(unsigned, __builtin_convertvector(v, bf2));
}
__device__ __forceinline__ u2 pk4(f4 v) { return u2{pk(v.x, v.y), pk(v.z, v.w)}; }
__device__ __forceinline__ float phi(float t) { return (t > 0.f ? t + 1.f : __builtin_amdgcn_exp2f(t * 1.44269504089f)) + 1e-6f; }
__device__ __forceinline__ f4 phi4(f4 v) { return f4{phi(v.x), phi(v.y), phi(v.z), phi(v.w)}; }
__device__ __forceinline__ u2 swap16(unsigned a, unsigned b) {
  auto r = __builtin_amdgcn_permlane16_swap(a, b, false, false);
  return u2{r[0], r[1]};
}
__device__ __forceinline__ u2 swap32(unsigned a, unsigned b) {
  auto r = __builtin_amdgcn_permlane32_swap(a, b, false, false);
  return u2{r[0], r[1]};
}
__device__ __forceinline__ float asf(unsigned x) { return __builtin_bit_cast(float, x); }
__device__ __forceinline__ unsigned asu(float x) { return __builtin_bit_cast(unsigned, x); }
__device__ __forceinline__ void flag_store(char* ws, int off, u64 tag) {
  __hip_atomic_store((u64*)(ws + off), tag, __ATOMIC_RELAXED, __HIP_MEMORY_SCOPE_AGENT);
}
__device__ __forceinline__ void poll(char* ws, int off, bool act, u64 tag) {
#pragma unroll 1
  for (int it = 0; it < SPIN_MAX; ++it) {
    u64 v = __hip_atomic_load((u64*)(ws + off), __ATOMIC_RELAXED, __HIP_MEMORY_SCOPE_AGENT);
    if (__builtin_amdgcn_ballot_w64(act && v != tag) == 0) break;
    __builtin_amdgcn_s_sleep(2);
  }
  asm volatile("" ::: "memory");
}
#define STAMP(i)

__global__ __launch_bounds__(512, 2) void fused_kernel(const void* __restrict__ px, const void* __restrict__ pwa,
                                                       const void* __restrict__ pwoa, const void* __restrict__ pwb,
                                                       const void* __restrict__ pwob, const void* __restrict__ pgw,
                                                       char* __restrict__ out, char* __restrict__ ws) {
  const int tid = threadIdx.x, l = tid & 63, w = __builtin_amdgcn_readfirstlane(tid >> 6);
  const int bid = blockIdx.x, xcd = bid & 7, idx = bid >> 3;
  {
    u64 pc; unsigned sz;
    asm volatile("s_getpc_b64 %0\n.Lfk_start:\n s_mov_b32 %1, .Lfk_end-.Lfk_start" : "=s"(pc), "=s"(sz));
    unsigned o = (unsigned)((((2 * idx + w) & 7) * 64 + l) * 128);
    if (w < 2 && o < sz)
      asm volatile("s_mov_b32 m0, %2\n\ts_nop 0\n\tglobal_load_lds_dword %0, %1" ::"v"(o), "s"(pc), "s"(DBGL + 256) : "memory");
  }
  const int r = l & 15, g = l >> 4, q = r >> 2, p = r & 3, mw = w & 3, nw = w >> 2;
  const int cq = xcd >> 1, tt = 8 * (xcd & 1) + (idx & 7), cs = idx >> 3;
  const bool isB = cs >= 2;
  const int pair = 4 * cq + 2 * (cs & 1) + ((cs < 2 ? 1 : 0) ^ (cq & 1));
  const int rot = (idx * 3 + (idx >> 3)) % 10;
  const u4 rX = mkrs(px, 2048 * 1280), rWA = mkrs(pwa, 640 * 3840), rWB = mkrs(pwb, 640 * 3840);
  const u4 rW1 = mkrs(isB ? pwb : pwa, 640 * 3840), rWSd = mkrs(ws, WS_BYTES);
  const rsrc_t rWS = __builtin_amdgcn_make_buffer_rsrc((void*)ws, 0, WS_BYTES, 0x00020000);
  STAMP(0);
  int xv0, xv1, wv[3];
  {
    int row = 16 * w + (l >> 3);
    xv0 = row * 1280 + (((l & 7) ^ ((row >> 1) & 7)) << 4);
    xv1 = (xv0 ^ 64) + 8 * 1280;
    const int base0 = isB ? 1280 + 160 * (2 * cq + cs - 2) : 1280 + 1280 * cs + 320 * cq;
    const int delta = isB ? 1120 : 0;
#pragma unroll
    for (int i = 0; i < 3; ++i) {
      int ci = 64 * i + l, rw = ci / 20, pos = ci - rw * 20;
      int t = pos ^ (2 * (w & 1));
      int c = t >= 10 ? t ^ 1 : t;
      wv[i] = (8 * w + rw) * 3840 + base0 + 16 * c + (c >= 10 ? delta : 0);
    }
  }
  const int xs0 = tt * (128 * 1280);
  auto kchunk = [&](int s) { int k = s + rot; return k >= 10 ? k - 10 : k; };
  auto issx = [&](int s, int i) { dma<64, 0>(rX, XR + (s & 3) * 16384 + w * 2048 + i * 1024, i ? xv1 : xv0, xs0 + kchunk(s) * 128); };
  auto issw = [&](int s, int i) {
    int wd = WR + ((s + 2) % 3) * 20480 + w * 2560 + i * 1024, so = kchunk(s) * 245760;
    if (i < 2) dma<64, 0>(rW1, wd, wv[i], so); else dma<32, 0>(rW1, wd, wv[2], so);
  };
#pragma unroll
  for (int s = 0; s < 3; ++s) { issx(s, 0); issx(s, 1); issw(s, 0); issw(s, 1); issw(s, 2); }
  SB();
  u64 tag;
  asm volatile("s_load_dwordx2 %0, %1, 0x0" : "=s"(tag) : "s"(ws) : "memory");
  int qva, qvb;
  {
    int pr = l / 5, c = l - pr * 5;
    int kb = 8 * w + (pr >> 1);
    qva = (kb + 4 * (pr & 1)) * 3840 + 80 * pair + 16 * c;
    qvb = (kb + 4 * ((pr & 1) ^ 1)) * 3840 + 80 * pair + 16 * c;
  }
  auto iss2 = [&](int rs, int i) {
    dma<40, 0>(i ? rWB : rWA, CR + (rs % 9) * 10240 + w * 640 + i * 5120, i ? qvb : qva, kchunk((rs + 6) % 10) * 245760);
  };
  auto issue2 = [&](int rs) { iss2(rs, 0); iss2(rs, 1); };
  int wa[5];
#pragma unroll
  for (int nt = 0; nt < 5; ++nt) {
    int byteA = 160 * nw + 32 * nt + 8 * p;
    int byteB = nt < 4 ? 160 * (nt >> 1) + 80 * nw + 40 * (nt & 1) + 8 * p : 160 * (p >> 1) + 80 * nw + 40 * (p & 1) + 32;
    int by = isB ? byteB : byteA;
    if (by >= 160) by ^= 16;
    by ^= (g & 1) << 5;
    wa[nt] = WR + (8 * g + q) * 320 + by;
  }
  const int xa0 = XR + (32 * mw + r) * 128 + ((g ^ (r >> 1)) << 4), xa1 = xa0 ^ 64;
  if (tid < 4) *(AS3 unsigned*)(size_t)(MISC + 4 * tid) = 0;

  u4 xk[10][2][2];
  u4 wf[2][5];
  f4 acc[2][5];
#pragma unroll
  for (int i = 0; i < 5; ++i) acc[0][i] = acc[1][i] = f4{0, 0, 0, 0};
  auto rdx = [&](int s, int kk) {
    int xo = (s & 3) * 16384;
    xk[s][kk][0] = lr128((kk ? xa1 : xa0) + xo);
    xk[s][kk][1] = lr128((kk ? xa1 : xa0) + xo + 2048);
  };
  auto rdw = [&](int s, int kk, int nt) {
    int wo = ((s + 2) % 3) * 20480 + kk * 10240;
    wf[kk][nt] = cat(ltr(wa[nt] + wo), ltr(wa[nt] + wo + 1280));
  };
  auto mm1 = [&](int s, int kk, int nt) {
    acc[0][nt] = mf(xk[s][kk][0], wf[kk][nt], acc[0][nt]);
    acc[1][nt] = mf(xk[s][kk][1], wf[kk][nt], acc[1][nt]);
  };
  int gv;
  {
    int ci = 10 * w + l, kc = (ci >> 3) + 6 + rot;
    kc = kc >= 20 ? kc - 20 : (kc >= 10 ? kc - 10 : kc);
    gv = kc * 128 + (ci & 7) * 16;
  }
  SB();
  asm volatile("s_waitcnt vmcnt(10) lgkmcnt(0)" : "+s"(tag)::"memory");
  tag += 1;
  bar();
  STAMP(1);
  rdx(0, 0);
#pragma unroll
  for (int nt = 0; nt < 5; ++nt) rdw(0, 0, nt);
  SB();
#pragma unroll
  for (int s = 0; s < 10; ++s) {
    rdx(s, 1); rdw(s, 1, 0); mm1(s, 0, 0); SB();
    rdw(s, 1, 1); rdw(s, 1, 2); mm1(s, 0, 1); SB();
    rdw(s, 1, 3); rdw(s, 1, 4); mm1(s, 0, 2); SB();
    if (s <= 6) issx(s + 3, 0);
    if (s == 7) dma<10, 0>(mkrs(pgw, 1280), GATE + 160 * w, gv, 0);
    if (s == 8) iss2(1, 1);
    if (s == 9) iss2(3, 1);
    mm1(s, 0, 3); SB();
    if (s <= 6) issx(s + 3, 1);
    if (s == 7) iss2(0, 0);
    if (s == 8) iss2(2, 0);
    if (s == 9) iss2(4, 0);
    mm1(s, 0, 4); SB();
    waitl();
    if (s < 8) waitv<7>();
    if (s == 8) waitv<6>();
    bar(); SB();
    if (s < 9) { rdx(s + 1, 0); rdw(s + 1, 0, 0); }
    mm1(s, 1, 0); SB();
    if (s < 9) { rdw(s + 1, 0, 1); rdw(s + 1, 0, 2); }
    mm1(s, 1, 1); SB();
    if (s < 9) { rdw(s + 1, 0, 3); rdw(s + 1, 0, 4); }
    if (s <= 6) issw(s + 3, 0);
    mm1(s, 1, 2); SB();
    if (s <= 6) issw(s + 3, 1);
    if (s == 7) iss2(0, 1);
    if (s == 8) iss2(2, 1);
    if (s == 9) iss2(4, 1);
    mm1(s, 1, 3); SB();
    if (s <= 6) issw(s + 3, 2);
    if (s == 7) iss2(1, 0);
    if (s == 8) iss2(3, 0);
    mm1(s, 1, 4); SB();
  }
  STAMP(2);
  if (!isB) {
    const bool isV = cs == 1;
#pragma unroll
    for (int nt = 0; nt < 5; ++nt) {
      int col = 80 * nw + 16 * nt + r, hl = col / 20, d = col - hl * 20;
      u2 lo = pk4(acc[0][nt]), hi = pk4(acc[1][nt]);
      bool sw = !isV && ((d >> 2) & 1);
      u4 v = sw ? cat(hi, lo) : cat(lo, hi);
      int ch = isV ? g ^ (((d >> 3) & 1) << 1) : g;
      int off = (hl >> 1) * (64 * 5120) + (hl & 1) * 1280 + d * 64 + ch * 16;
      __builtin_amdgcn_raw_buffer_store_b128(v, rWS, off + WS_KV + 4 * cq * (64 * 5120) + (4 * tt + mw) * 5120 + (isV ? 2560 : 0), 0, 16);
    }
    issue2(5); issue2(6); issue2(7); issue2(8);
    waitv<8 + 5 + 8>();
  } else {
    u4 k0 = cat(pk4(phi4(acc[0][0])), pk4(phi4(acc[1][0])));
    u4 k1 = cat(pk4(phi4(acc[0][1])), pk4(phi4(acc[1][1])));
    u4 kr = cat(pk4(phi4(acc[0][4])), pk4(phi4(acc[1][4])));
    u4 v0 = cat(pk4(acc[0][2]), pk4(acc[1][2]));
    u4 v1 = cat(pk4(acc[0][3]), pk4(acc[1][3]));
    u4 vr = cat(pk4(acc[0][4]), pk4(acc[1][4]));
    const unsigned one2 = 0x3F803F80u;
    if (r < 8) vr = u4{one2, one2, one2, one2};
    u4 kh;
#pragma unroll
    for (int i = 0; i < 4; ++i) kh[i] = __builtin_amdgcn_update_dpp(0u, kr[i], 0x118, 0xf, 0xf, true);
    const f4 z = {0, 0, 0, 0};
    f4 n0 = mf(k0, v0, z), n1 = mf(k1, v1, z), m2 = mf(k0, vr, z), m3 = mf(k1, vr, z);
    f4 m4 = mf(kr, v0, z), m5 = mf(kr, v1, z), m6 = mf(kh, vr, z);
    f4 n2 = (q & 1) ? m3 : m2;
    f4 n3 = g == 0 ? m4 : (g == 1 ? m5 : m6);
    int sa = SCR + w * 4096 + l * 16;
    lw128(sa, __builtin_bit_cast(u4, n0)); lw128(sa + 1024, __builtin_bit_cast(u4, n1));
    lw128(sa + 2048, __builtin_bit_cast(u4, n2)); lw128(sa + 3072, __builtin_bit_cast(u4, n3));
    waitl(); bar();
    int ra = SCR + nw * 16384 + mw * 1024 + l * 16;
    f4 s0 = __builtin_bit_cast(f4, lr128(ra)), s1 = __builtin_bit_cast(f4, lr128(ra + 4096));
    f4 s2 = __builtin_bit_cast(f4, lr128(ra + 8192)), s3 = __builtin_bit_cast(f4, lr128(ra + 12288));
    f4 sm = (s0 + s1) + (s2 + s3);
    int pb = 2 * (2 * cq + cs - 2) + nw;
    __builtin_amdgcn_raw_buffer_store_b128(__builtin_bit_cast(u4, sm), rWS, (l >> 5) * 8192 + (l & 31) * 16 +
                                           WS_KVP + pb * 65536 + mw * 16384 + tt * 512, 0, 16);
    bar();
    issue2(5); issue2(6); issue2(7); issue2(8);
    waitv<8 + 1 + 8>();
  }
  bar();
  STAMP(3);
  f4 qa[2], qb[2], qr[2];
  qa[0] = qa[1] = qb[0] = qb[1] = qr[0] = qr[1] = f4{0, 0, 0, 0};
  u4 gth[8];
  {
    const int a0 = CR + (8 * g + 2 * q) * 80 + 40 * nw + 8 * p;
    const int rb = CR + (8 * g + 2 * q) * 80 + 40 * nw + 32;
    const int r0 = rb + ((p & 1) ? 5120 + 80 : 0), r1 = rb + ((p & 1) ? 5120 : 80);
    const int ga = GATE + g * 16;
    u4 fa[2], fb[2], fr[2], fg[2], xq[2][2];
    auto rd2 = [&](int rs, int kk) {
      int o = (rs % 9) * 10240 + kk * 2560;
      fa[kk] = cat(ltr(a0 + o), ltr(a0 + o + 80));
      fb[kk] = cat(ltr(a0 + o + 5120 + 80), ltr(a0 + o + 5120));
      fr[kk] = cat(ltr(r0 + o), ltr(r1 + o));
      fg[kk] = lr128(ga + rs * 128 + kk * 64);
      if (rs < 4) {
        int xo = ((rs + 6) & 3) * 16384;
        xq[kk][0] = lr128((kk ? xa1 : xa0) + xo);
        xq[kk][1] = lr128((kk ? xa1 : xa0) + xo + 2048);
      }
    };
    auto mm2 = [&](int rs, int kk) {
      u4 x0 = rs < 4 ? xq[kk][0] : xk[rs >= 4 ? rs - 4 : 0][kk][0];
      u4 x1 = rs < 4 ? xq[kk][1] : xk[rs >= 4 ? rs - 4 : 0][kk][1];
      u4 f = r >= 8 ? fg[kk] : fr[kk];
      qa[0] = mf(fa[kk], x0, qa[0]); qa[1] = mf(fa[kk], x1, qa[1]);
      qb[0] = mf(fb[kk], x0, qb[0]); qb[1] = mf(fb[kk], x1, qb[1]);
      qr[0] = mf(f, x0, qr[0]); qr[1] = mf(f, x1, qr[1]);
    };
    rd2(0, 0);
#pragma unroll
    for (int rs = 0; rs < 10; ++rs) {
      rd2(rs, 1);
      mm2(rs, 0);
      waitl();
      if (rs <= CHK) {
        if (rs == CHK) waitvn(0);
        else if (rs == 0) waitvn(PUB == 0 ? 8 : 15);
        else if (rs < 4) waitvn(rs < PUB ? 2 * (3 - rs) + 11 : 10);
        else waitvn(rs < 8 ? 2 * (8 - rs) : 0);
        if (rs == CHK) {
          if (w == 0) {
            int t2 = tt - 2 + (l & 3);
            bool a1 = l < 8 && (l & 3) < 3 && t2 >= 0;
            int off = l < 8 ? WS_F1 + ((l >> 2) * 64 + cq * 16 + (t2 < 0 ? 0 : t2)) * 8 : WS_F2 + ((pair >> 1) * 16 + (l & 15)) * 8;
            poll(ws, off, a1 || (l >= 16 && l < 32), tag);
          }
        }
        bar();
      }
      if (rs == 0) issue2(9);
      if (rs == PUB && tid == 0) flag_store(ws, isB ? WS_F2 + ((2 * cq + cs - 2) * 16 + tt) * 8 : WS_F1 + (cs * 64 + cq * 16 + tt) * 8, tag);
      if (rs == CHK) {
        STAMP(4);
#pragma unroll
        for (int i = 0; i < 8; ++i) {
          int n = 8 * i + w;
          if (n < 60) {
            int b = n / 5, pc = n - 5 * b, gb = 4 * (tt - 2) + b;
            gb = gb < 0 ? 4 * tt : gb;
            dma<64, 1>(rWSd, b * KVPITCH + pc * 1024, l * 16, WS_KV + pair * (64 * 5120) + gb * 5120 + pc * 1024);
          }
        }
#pragma unroll
        for (int i = 0; i < 8; ++i)
          gth[i] = __builtin_amdgcn_raw_buffer_load_b128(rWS, l * 16, WS_KVP + pair * 65536 + w * 8192 + i * 1024, 16);
        if (tid < 24) {
          int b = tid >> 1;
          unsigned v = (tid & 1) ? 0x3F803F80u : (4 * (tt - 2) + b < 0 ? 0xF149F149u : 0u);
          lw128(b * KVPITCH + 5120 + (tid & 1) * 16, u4{v, v, v, v});
        }
      }
      if (rs < 9) rd2(rs + 1, 0);
      mm2(rs, 1);
    }
  }
  STAMP(5);
  {
    f4 s = __builtin_bit_cast(f4, gth[0]);
#pragma unroll
    for (int i = 1; i < 8; ++i) s += __builtin_bit_cast(f4, gth[i]);
    f4 o;
#pragma unroll
    for (int i = 0; i < 4; ++i) { u2 t = swap32(asu(s[i]), asu(s[i])); o[i] = asf(t.x) + asf(t.y); }
    if (l < 32) lw64(KF + w * 256 + l * 8, pk4(o));
  }
  float gate[2];
  u4 qf[2], pf[2];
#pragma unroll
  for (int mt = 0; mt < 2; ++mt) {
    float lg = asf(swap32(asu(qr[mt][0]), asu(qr[mt][0])).y);
    gate[mt] = __builtin_amdgcn_rcpf(1.f + __builtin_amdgcn_exp2f(-1.44269504089f * lg));
    u2 rr = pk4(qr[mt]);
    u2 up = g == 0 ? rr : (g == 1 ? u2{0x3F80u, 0u} : u2{0u, 0u});
    qf[mt] = cat(pk4(qa[mt]), up);
    u2 pr = pk4(phi4(qr[mt]));
    pf[mt] = cat(pk4(phi4(qb[mt])), g == 1 ? pr : u2{0u, 0u});
  }
  waitv<0>(); waitl(); bar();
  STAMP(6);
  const int ov = (g & 1) * 20480 + (2 * nw + (g >> 1)) * 256 + r * 16;
  const int ovr = (g & 1) * 20480 + 16384 + (pair >> 2) * 1024 + (pair & 3) * 256 + nw * 128 + r * 8;
  const int os = (8 * tt + 2 * mw) * 20480;
  auto store_o = [&](f4 m0, f4 m1, f4 e0, f4 e1, int base) {
    u2 a = pk4(m0), b = pk4(m1);
    u2 t0 = swap16(a.x, b.x), t1 = swap16(a.y, b.y);
    __builtin_amdgcn_raw_buffer_store_b128(u4{t0.x, t1.x, t0.y, t1.y}, rWS, ov + base + os + pair * 1024, 0, 16);
    u2 c = pk4(e0), d = pk4(e1);
    u2 y = u2{swap16(c.x, d.x).x, swap16(c.y, d.y).x};
    if (l < 32) __builtin_amdgcn_raw_buffer_store_b64(y, rWS, ovr, base + os, 16);
  };
  {
    int rp = r < 4 ? 8 + 4 * nw + r : 4 * nw;
    u4 a0 = cat(lr64(KF + nw * 512 + l * 8), lr64(KF + 1536 + (16 * nw + r) * 8));
    u4 a1 = cat(lr64(KF + 1024 + (16 * g + rp) * 8), lr64(KF + 1536 + (32 + 16 * nw + rp) * 8));
    const f4 z = {0, 0, 0, 0};
    f4 m[2], e[2];
#pragma unroll
    for (int mt = 0; mt < 2; ++mt) {
      m[mt] = mf(a0, pf[mt], z);
      e[mt] = mf(a1, pf[mt], z);
      float den = asf(swap16(asu(e[mt][0]), asu(e[mt][0])).y) + 1e-6f;
      float sc = (1.f - gate[mt]) * __builtin_amdgcn_rcpf(den);
      m[mt] *= sc; e[mt] *= sc;
    }
    store_o(m[0], m[1], e[0], e[1], WS_OB);
  }
  const int u = 4 * xcd + (idx >> 3), cb = idx & 7;
  const u4 rOA = mkrs(pwoa, 640 * 1280), rOB = mkrs(pwob, 640 * 1280);
  int ovm, ove;
  {
    int pr = l / 10, c = l - 10 * pr;
    int rho = 16 * (w >> 2) + 8 * (w & 1) + 4 * ((w >> 1) & 1) + pr;
    ovm = (20 * (rho >> 4) + (rho & 15)) * 1280 + 160 * cb + 16 * c;
    ove = (20 * (rho >> 2) + 16 + (rho & 3)) * 1280 + 160 * cb + 16 * c;
  }
  auto issue3 = [&](u4 rs, int kh, int i, int slot) {
    if (i < 8) dma<40, 0>(rs, slot * 5120 + w * 640, ovm, (8 * kh + i) * 51200);
    else dma<40, 0>(rs, slot * 5120 + w * 640, ove, (2 * kh + i - 8) * 204800);
  };
  {
#pragma unroll
    for (int i = 0; i < 9; ++i) { issue3(rOB, 0, i, 13 + i); issue3(rOB, 1, i, 22 + i); }
  }
  STAMP(7);
  {
    const int kb = mw * KVPITCH + nw * 1280;
    const int ka0 = kb + (4 * g + q) * 64 + p * 16 + 8 * (g & 1), ka1 = ka0 ^ 8;
    const int kr0 = (g & 1) ? mw * KVPITCH + 5120 + 8 : kb + (16 + q) * 64 + p * 16, kr1 = kr0 ^ 8;
    f4 S[2][18];
    const f4 z = {0, 0, 0, 0};
#pragma unroll
    for (int t = 0; t < 18; ++t) {
      int o = (t >> 1) * KVPITCH;
      u4 kf = cat(ltr(((t & 1) ? ka1 : ka0) + o), ltr(((t & 1) ? kr1 : kr0) + o));
      if (t < 17) S[0][t] = mf(kf, qf[0], z);
      if (t > 0) S[1][t] = mf(kf, qf[1], z);
    }
    waitv<18>();
    if (l == 0) {
      unsigned c = __hip_atomic_fetch_add((AS3 unsigned*)(size_t)MISC, 1u, __ATOMIC_RELAXED, __HIP_MEMORY_SCOPE_WORKGROUP);
      if (c == 7) flag_store(ws, WS_F3B + (tt * 16 + pair) * 8, tag);
    }
    const float C = 0.22360679775f * 1.44269504089f;
    u2 P[2][18];
    unsigned mk0 = (4 * g <= r ? 0xFFFFu : 0u) | (4 * g + 1 <= r ? 0xFFFF0000u : 0u);
    unsigned mk1 = (4 * g + 2 <= r ? 0xFFFFu : 0u) | (4 * g + 3 <= r ? 0xFFFF0000u : 0u);
#pragma unroll
    for (int mt = 0; mt < 2; ++mt) {
      f4 sm;
#pragma unroll
      for (int i = 0; i < 4; ++i) sm[i] = (4 * g + i <= r) ? S[mt][16 + mt][i] : S[mt][mt][i];
      S[mt][mt] = sm;
      float mx = fmaxf(fmaxf(sm[0], sm[1]), fmaxf(sm[2], sm[3]));
#pragma unroll
      for (int t = 1; t < 16; ++t) {
        f4 s = S[mt][mt + t];
        mx = fmaxf(fmaxf(mx, s[0]), s[1]);
        mx = fmaxf(fmaxf(mx, s[2]), s[3]);
      }
      u2 t16 = swap16(asu(mx), asu(mx));
      mx = fmaxf(asf(t16.x), asf(t16.y));
      u2 t32 = swap32(asu(mx), asu(mx));
      mx = fmaxf(asf(t32.x), asf(t32.y));
      const float nm = -mx * C;
#pragma unroll
      for (int t = 0; t < 16; ++t) {
        f4 s = S[mt][mt + t] * C + nm;
        f4 e = {__builtin_amdgcn_exp2f(s[0]), __builtin_amdgcn_exp2f(s[1]), __builtin_amdgcn_exp2f(s[2]), __builtin_amdgcn_exp2f(s[3])};
        P[mt][mt + t] = pk4(e);
      }
      u2 pm = P[mt][mt];
      P[mt][16 + mt] = u2{pm.x & mk0, pm.y & mk1};
      P[mt][mt] = u2{pm.x & ~mk0, pm.y & ~mk1};
      P[mt][17 - 17 * mt] = u2{0u, 0u};
    }
    const int vb = mw * KVPITCH + (2 + nw) * 1280;
    const int va0 = vb + r * 64 + ((g ^ (((r >> 3) & 1) << 1)) << 4);
    const int va1 = r < 4 ? vb + (16 + r) * 64 + g * 16 : mw * KVPITCH + 5136;
    f4 m[2] = {z, z}, e[2] = {z, z};
#pragma unroll
    for (int i = 0; i < 9; ++i) {
      u4 v0 = lr128(va0 + i * KVPITCH), v1 = lr128(va1 + i * KVPITCH);
#pragma unroll
      for (int mt = 0; mt < 2; ++mt) {
        u4 pp = cat(P[mt][2 * i], P[mt][2 * i + 1]);
        m[mt] = mf(v0, pp, m[mt]);
        e[mt] = mf(v1, pp, e[mt]);
      }
    }
#pragma unroll
    for (int mt = 0; mt < 2; ++mt) {
      float den = asf(swap16(asu(e[mt][0]), asu(e[mt][0])).y);
      float sc = gate[mt] * __builtin_amdgcn_rcpf(den);
      m[mt] *= sc; e[mt] *= sc;
    }
    store_o(m[0], m[1], e[0], e[1], WS_OA);
  }
  STAMP(8);
  waitv<0>();
  if (l == 0) {
    unsigned c = __hip_atomic_fetch_add((AS3 unsigned*)(size_t)(MISC + 4), 1u, __ATOMIC_RELAXED, __HIP_MEMORY_SCOPE_WORKGROUP);
    if (c == 7) flag_store(ws, WS_F3A + (tt * 16 + pair) * 8, tag);
  }
  if (w == 0) poll(ws, WS_F3B + ((u >> 1) * 16 + (l & 15)) * 8, true, tag);
  waitl(); bar();
  STAMP(9);
  {
    const int pa = (16 * (g >> 1) + 4 * (g & 1) + q) * 160 + 8 * p;
    const int pa13 = pa + (13 + 9 * nw) * 5120;
    const int ob = (4 * u + mw) * 20480;
    f4 oc[5];
#pragma unroll
    for (int i = 0; i < 5; ++i) oc[i] = f4{0, 0, 0, 0};
    u4 of[8];
    u2 orl[2], orh[2];
    auto ldo = [&](int base) {
#pragma unroll
      for (int i = 0; i < 8; ++i) of[i] = bld128(rWSd, l * 16, base + ob + (8 * nw + i) * 1024);
#pragma unroll
      for (int i = 0; i < 2; ++i) {
        int so = base + ob + 16384 + (2 * nw + i) * 1024;
        orl[i] = bld64<0>(rWSd, g * 256 + r * 8, so);
        orh[i] = bld64<128>(rWSd, g * 256 + r * 8, so);
      }
    };
    auto step = [&](int a, int i, int later) {
      u4 wfr[5];
#pragma unroll
      for (int nt = 0; nt < 5; ++nt) wfr[nt] = cat(ltr(a + 32 * nt), ltr(a + 32 * nt + 1280));
      if (i < 8) waitv_on(11 - i + later, of[i]); else waitv_on(2 * (9 - i) + later, orl[i - 8], orh[i - 8]);
      u4 o = i < 8 ? of[i] : cat(orl[i - 8], orh[i - 8]);
#pragma unroll
      for (int nt = 0; nt < 5; ++nt) oc[nt] = mf(wfr[nt], o, oc[nt]);
    };
    ldo(WS_OB);
    issue3(rOB, 0, 9, 0); issue3(rOB, 1, 9, 1);
#pragma unroll
    for (int i = 0; i < 5; ++i) { issue3(rOA, 0, i, 2 + i); issue3(rOA, 1, i, 7 + i); }
#pragma unroll
    for (int i = 0; i < 9; ++i) step(pa13 + i * 5120, i, 12);
    waitl(); waitv<10>(); bar();
    STAMP(10);
#pragma unroll
    for (int i = 5; i < 10; ++i) { issue3(rOA, 0, i, 13 + i - 5); issue3(rOA, 1, i, 22 + i - 5); }
    step(pa + nw * 5120, 9, 20);
    SB();
    waitv<0>();
    if (w == 0) poll(ws, WS_F3A + ((u >> 1) * 16 + (l & 15)) * 8, true, tag);
    bar();
    STAMP(11);
    ldo(WS_OA);
#pragma unroll
    for (int i = 0; i < 5; ++i) step(pa + (2 + 5 * nw + i) * 5120, i, 0);
#pragma unroll
    for (int i = 5; i < 10; ++i) step(pa13 + (i - 5) * 5120, i, 0);
    STAMP(12);
    const int xa = 18 * 5120 + mw * 5120 + l * 16;
    if (nw) { lw128(xa, __builtin_bit_cast(u4, oc[0])); lw128(xa + 1024, __builtin_bit_cast(u4, oc[1])); }
    else { lw128(xa + 2048, __builtin_bit_cast(u4, oc[2])); lw128(xa + 3072, __builtin_bit_cast(u4, oc[3])); lw128(xa + 4096, __builtin_bit_cast(u4, oc[4])); }
    waitl(); bar();
    const rsrc_t rO = __builtin_amdgcn_make_buffer_rsrc((void*)out, 0, 2048*1280, 0x00020000); const int oo = (64 * u + 16 * mw + r) * 1280 + 160 * cb + 8 * g;
    if (nw) {
#pragma unroll
      for (int nt = 2; nt < 5; ++nt) __builtin_amdgcn_raw_buffer_store_b64(pk4(oc[nt] + __builtin_bit_cast(f4, lr128(xa + 1024 * nt))), rO, oo + 32 * nt, 0, 16);
    } else {
#pragma unroll
      for (int nt = 0; nt < 2; ++nt) __builtin_amdgcn_raw_buffer_store_b64(pk4(oc[nt] + __builtin_bit_cast(f4, lr128(xa + 1024 * nt))), rO, oo + 32 * nt, 0, 16);
    }
  }
  STAMP(13);
  if (bid == 0 && w == 0) {
#pragma unroll 1
    for (int i = 0; i < 4; ++i) poll(ws, WS_F3B + (i * 64 + l) * 8, true, tag);
    if (l == 0) flag_store(ws, 0, tag);
  }
  asm volatile(".Lfk_end:" ::: "memory");
}

extern "C" void kernel_launch(void* const* d_in, const int* in_sizes, int n_in, void* d_out, int out_size, void* d_ws,
                              size_t ws_size, hipStream_t stream) {
  (void)hipFuncSetAttribute((const void*)fused_kernel, hipFuncAttributeMaxDynamicSharedMemorySize, LDS_BYTES);
  fused_kernel<<<256, 512, LDS_BYTES, stream>>>(d_in[0], d_in[1], d_in[2], d_in[4], d_in[5], d_in[6], (char*)d_out, (char*)d_ws);
}
```

```cpp
#include <hip/hip_runtime.h>
#include <stdint.h>
typedef __bf16 bf8 __attribute__((ext_vector_type(8)));
typedef __bf16 bf2 __attribute__((ext_vector_type(2)));
typedef float f4 __attribute__((ext_vector_type(4)));
typedef float f2 __attribute__((ext_vector_type(2)));
typedef float f16v __attribute__((ext_vector_type(16)));
typedef unsigned u4 __attribute__((ext_vector_type(4)));
typedef unsigned u2 __attribute__((ext_vector_type(2)));
typedef short s4 __attribute__((ext_vector_type(4)));
typedef unsigned long long u64;
#define AS3 __attribute__((address_space(3)))
typedef __amdgpu_buffer_rsrc_t rsrc_t;

constexpr int SLOT = 36864, WOFF = 16384, W2L = 137216, VL = 126976, BP = 2688, HP = 1344, TQ = 32768, WOL = 4096;
constexpr int DUMP = 159232, GATE = 159744, KF = GATE + 1280, WD = KF + 2560, LDS_BYTES = 163840;
constexpr int PW = WD, CD = WD + 16, KR = WD + 20, VR = WD + 24, KFR = WD + 28, QR = WD + 32, RB = WD + 48, KD = WD + 64, VD = WD + 68,
              WA = WD + 72, CP = WD + 76, WL = WD + 80, CE = WD + 84, VLD = WD + 88, PB = WD + 96, EX = WD + 112;
constexpr int WS_FA = 2048, WS_FL = 4096, WS_DBG = 65536;
constexpr int WS_W2 = 1 << 20, WS_WO = 3 << 20, WS_K = 5 << 20, WS_V = 8 << 20;
constexpr int WS_OB = 11 << 20, WS_OA = WS_OB + 2621440, WS_KVP = WS_OA + 2621440, WS_BYTES = 18 << 20;
#define SPIN 100000
constexpr int SPIN_MAX = SPIN;
#define DEPTH 4
#define SK(s) (s)
#define PRE 2
#define RAMP 2
constexpr int LASTI(int s) {
  int a = PRE - 1 + RAMP * (s + 1), b = s + DEPTH - 1;
  if (b < DEPTH - 1) b = DEPTH - 1;
  int m = a < b ? a : b;
  return m > 9 ? 9 : m;
}

template <int N> __device__ __forceinline__ void waitv() { asm volatile("s_waitcnt vmcnt(%0)" ::"i"(N) : "memory"); }
__device__ __forceinline__ void waitvn(int n) { asm volatile("s_waitcnt vmcnt(%0)" ::"i"(n) : "memory"); }
__device__ __forceinline__ void fence() { asm volatile("" ::: "memory"); }
__device__ __forceinline__ void waitl() { fence(); __builtin_amdgcn_s_waitcnt(0xC07F); fence(); }
__device__ __forceinline__ void bar() { fence(); __builtin_amdgcn_s_barrier(); fence(); }
__device__ __forceinline__ void dma(u4 rs, int lds, int voff, int soff) {
  asm volatile("s_mov_b32 m0, %0\n\ts_nop 0\n\tbuffer_load_dwordx4 %1, %2, %3 offen lds" ::"s"(lds), "v"(voff), "s"(rs), "s"(soff) : "memory");
}
template <int N> __device__ __forceinline__ void dmas(u4 rs, int lds, int voff, int soff) {
  asm volatile("s_mov_b32 m0, %0\n\ts_nop 0\n\tbuffer_load_dwordx4 %1, %2, %3 offen sc1 lds" ::"s"(lds), "v"(voff), "s"(rs), "s"(soff) : "memory");
  if constexpr (N > 1) asm volatile("buffer_load_dwordx4 %0, %1, %2 offen offset:1024 sc1 lds" ::"v"(voff), "s"(rs), "s"(soff) : "memory");
  if constexpr (N > 2) asm volatile("buffer_load_dwordx4 %0, %1, %2 offen offset:2048 sc1 lds" ::"v"(voff), "s"(rs), "s"(soff) : "memory");
  if constexpr (N > 3) asm volatile("buffer_load_dwordx4 %0, %1, %2 offen offset:3072 sc1 lds" ::"v"(voff), "s"(rs), "s"(soff) : "memory");
  if constexpr (N > 4) dmas<N - 4>(rs, lds + 4096, voff, soff + 4096);
}
__device__ __forceinline__ u4 mkrs(const void* p, unsigned bytes) {
  u64 a = (u64)p;
  return u4{(unsigned)a, (unsigned)(a >> 32) & 0xffffu, bytes, 0x00020000u};
}
__device__ __forceinline__ rsrc_t mkr(const void* p, int bytes) { return __builtin_amdgcn_make_buffer_rsrc((void*)p, 0, bytes, 0x00020000); }
#define SB() __builtin_amdgcn_sched_barrier(0)
__device__ __forceinline__ int opq(int x) { asm volatile("" : "+v"(x)); return x; }
#define LANES(t) const int l = (t) & 63, r = l & 15, g = l >> 4, q = r >> 2, p = r & 3, h = l >> 5, l16 = l * 16
__device__ __forceinline__ u4 lr128(int a) { return *(const AS3 u4*)(size_t)a; }
__device__ __forceinline__ u2 lr64(int a) { return *(const AS3 u2*)(size_t)a; }
__device__ __forceinline__ unsigned lr32(int a) { return *(const AS3 unsigned*)(size_t)a; }
__device__ __forceinline__ void lw128(int a, u4 v) { *(AS3 u4*)(size_t)a = v; }
__device__ __forceinline__ void lw64(int a, u2 v) { *(AS3 u2*)(size_t)a = v; }
__device__ __forceinline__ void lw32(int a, unsigned v) { *(AS3 unsigned*)(size_t)a = v; }
__device__ __forceinline__ u2 ltr(int a) {
  s4 v = __builtin_amdgcn_ds_read_tr16_b64_v4i16((AS3 s4*)(size_t)a);
  return __builtin_bit_cast(u2, v);
}
__device__ __forceinline__ void lset(int a, unsigned v) {
  unsigned t0, t1;
  asm volatile("v_mov_b32 %0, %2\n\tv_mov_b32 %1, %3\n\ts_mov_b64 exec, 1\n\tds_write_b32 %0, %1\n\ts_mov_b64 exec, -1"
               : "=&v"(t0), "=&v"(t1) : "s"(a), "i"(v) : "memory");
}
__device__ __forceinline__ void linc(int a) {
  unsigned t0, t1;
  asm volatile("v_mov_b32 %0, %2\n\tv_mov_b32 %1, 1\n\ts_mov_b64 exec, 1\n\tds_add_u32 %0, %1\n\ts_mov_b64 exec, -1"
               : "=&v"(t0), "=&v"(t1) : "s"(a) : "memory");
}
__device__ __forceinline__ void lwait(int a, unsigned v) {
  unsigned t0, t1, c, x;
  asm volatile("v_mov_b32 %0, %4\n\ts_mov_b32 %2, %6\n"
               "1:\n\tds_read_b32 %1, %0\n\ts_waitcnt lgkmcnt(0)\n\tv_readfirstlane_b32 %3, %1\n\ts_cmp_ge_u32 %3, %5\n\ts_cbranch_scc1 2f\n\t"
               "s_sub_u32 %2, %2, 1\n\ts_cbranch_scc1 2f\n\ts_sleep 1\n\ts_branch 1b\n2:"
               : "=&v"(t0), "=&v"(t1), "=&s"(c), "=&s"(x) : "s"(a), "i"(v), "i"(SPIN_MAX) : "memory", "scc");
}
__device__ __forceinline__ void lwait4(int a, unsigned v) {
  unsigned t0, t1, c;
  asm volatile("v_mbcnt_lo_u32_b32 %0, 7, 0\n\ts_mov_b32 %2, %5\n\tv_lshl_add_u32 %0, %0, 2, %3\n"
               "1:\n\tds_read_b32 %1, %0\n\ts_waitcnt lgkmcnt(0)\n\tv_cmp_gt_u32 vcc, %4, %1\n\ts_sub_u32 %2, %2, 1\n\ts_cbranch_vccz 2f\n\t"
               "s_cbranch_scc1 2f\n\ts_sleep 1\n\ts_branch 1b\n2:"
               : "=&v"(t0), "=&v"(t1), "=&s"(c) : "s"(a), "i"(v), "i"(SPIN_MAX) : "memory", "scc", "vcc");
}
__device__ __forceinline__ u4 cat(u2 a, u2 b) { return u4{a.x, a.y, b.x, b.y}; }
__device__ __forceinline__ f4 mf(u4 a, u4 b, f4 c) {
  return __builtin_amdgcn_mfma_f32_16x16x32_bf16(__builtin_bit_cast(bf8, a), __builtin_bit_cast(bf8, b), c, 0, 0, 0);
}
__device__ __forceinline__ f16v mf32(u4 a, u4 b, f16v c) {
  return __builtin_amdgcn_mfma_f32_32x32x16_bf16(__builtin_bit_cast(bf8, a), __builtin_bit_cast(bf8, b), c, 0, 0, 0);
}
__device__ __forceinline__ f16v mf8(u2 a, u2 b, f16v c) {
  return __builtin_amdgcn_mfma_f32_32x32x8bf16_1k(__builtin_bit_cast(s4, a), __builtin_bit_cast(s4, b), c, 0, 0, 0);
}
__device__ __forceinline__ unsigned pk(float a, float b) {
  f2 v = {a, b};
  return __builtin_bit_cast(unsigned, __builtin_convertvector(v, bf2));
}
__device__ __forceinline__ u2 pk4(f4 v) { return u2{pk(v.x, v.y), pk(v.z, v.w)}; }
__device__ __forceinline__ float ex2(float x) { return __builtin_amdgcn_exp2f(x); }
__device__ __forceinline__ float phi(float t) { return (t > 0.f ? t + 1.f : ex2(t * 1.44269504089f)) + 1e-6f; }
__device__ __forceinline__ f4 phi4(f4 v) { return f4{phi(v.x), phi(v.y), phi(v.z), phi(v.w)}; }
__device__ __forceinline__ u2 swap16(unsigned a, unsigned b) {
  auto r = __builtin_amdgcn_permlane16_swap(a, b, false, false);
  return u2{r[0], r[1]};
}
__device__ __forceinline__ u2 swap32(unsigned a, unsigned b) {
  auto r = __builtin_amdgcn_permlane32_swap(a, b, false, false);
  return u2{r[0], r[1]};
}
__device__ __forceinline__ u4 widen(u2 a, u2 b) {
  u2 t0 = swap16(a.x, b.x), t1 = swap16(a.y, b.y);
  return u4{t0.x, t1.x, t0.y, t1.y};
}
__device__ __forceinline__ float asf(unsigned x) { return __builtin_bit_cast(float, x); }
__device__ __forceinline__ unsigned asu(float x) { return __builtin_bit_cast(unsigned, x); }
__device__ __forceinline__ unsigned nosent(unsigned x) { return min(x, 0xFFFFFFFEu); }
__device__ __forceinline__ u4 nosent4(u4 x) { return u4{nosent(x.x), nosent(x.y), nosent(x.z), nosent(x.w)}; }
__device__ __forceinline__ unsigned max4(u4 x) { return max(max(x.x, x.y), max(x.z, x.w)); }
__device__ __forceinline__ void flag_store(char* ws, int off, u64 tag) {
  __hip_atomic_store((u64*)(ws + off), tag, __ATOMIC_RELAXED, __HIP_MEMORY_SCOPE_AGENT);
}
__device__ __forceinline__ void poll(char* ws, int off, bool act, u64 tag) {
#pragma unroll 1
  for (int it = 0; it < SPIN_MAX; ++it) {
    u64 v = __hip_atomic_load((u64*)(ws + off), __ATOMIC_RELAXED, __HIP_MEMORY_SCOPE_AGENT);
    if (__builtin_amdgcn_ballot_w64(act && v != tag) == 0) break;
    __builtin_amdgcn_s_sleep(2);
  }
  fence();
}
template <int N, int G, class F> __device__ __forceinline__ void gather(u4 (&o)[N][G], rsrc_t rs, int voff, F soff) {
  unsigned pend = (1u << N) - 1;
#pragma unroll 1
  for (int it = 0; it < SPIN_MAX; ++it) {
    fence();
#pragma unroll
    for (int i = 0; i < N; ++i)
      if (pend >> i & 1) {
#pragma unroll
        for (int j = 0; j < G; ++j) o[i][j] = __builtin_amdgcn_raw_buffer_load_b128(rs, voff, soff(i, j), 16);
      }
#pragma unroll
    for (int i = 0; i < N; ++i) {
      unsigned m = max4(o[i][0]);
#pragma unroll
      for (int j = 1; j < G; ++j) m = max(m, max4(o[i][j]));
      if (__builtin_amdgcn_ballot_w64(m == 0xFFFFFFFFu) == 0) pend &= ~(1u << i);
    }
    if (pend == 0) break;
    __builtin_amdgcn_s_sleep(1);
  }
  fence();
}
#define STAMP(i)
#define CSTAMP(i)

__global__ __launch_bounds__(512, 2) void fused_kernel(const void* __restrict__ px, const void* __restrict__ pwa,
                                                       const void* __restrict__ pwoa, const void* __restrict__ pwb,
                                                       const void* __restrict__ pwob, const void* __restrict__ pgw,
                                                       char* __restrict__ out, char* __restrict__ ws) {
  const int tid = threadIdx.x, w = __builtin_amdgcn_readfirstlane(tid >> 6), aw = w & 3;
  const int bid = blockIdx.x, xcd = bid & 7, idx = bid >> 3;
  const int cq = xcd >> 1, tt = 8 * (xcd & 1) + (idx & 7), cs = idx >> 3, pair = 4 * cq + cs;
  const int u = 4 * xcd + (idx >> 3), cb = idx & 7;
  const bool isB = cs >= 2;
  const u4 rWSd = mkrs(ws, WS_BYTES);
  const rsrc_t rWS = mkr(ws, WS_BYTES);
  const f4 z4 = {0, 0, 0, 0};
  const f16v z16 = {0, 0, 0, 0, 0, 0, 0, 0, 0, 0, 0, 0, 0, 0, 0, 0};
  u64 pc; unsigned sz;
  asm volatile("s_getpc_b64 %0\n.Lfk_start:\n s_mov_b32 %1, .Lfk_e1-.Lfk_start\n s_max_u32 %1, %1, .Lfk_e2-.Lfk_start" : "=s"(pc), "=s"(sz)::"scc");
  STAMP(0);
  auto finish = [&](f16v D, float num, float eps, int hh, int base, int l) {
    const int r = l & 15, g = l >> 4, h = l >> 5;
    float den = asf(swap32(asu(D[8]), asu(D[8])).y) + eps;
    float sc = num * __builtin_amdgcn_rcpf(den);
    int tb = base + (8 * tt + 2 * aw + (g & 1)) * 20480 + hh * 10240 + r * 16;
    u4 m = {pk(D[0] * sc, D[1] * sc), pk(D[2] * sc, D[3] * sc), pk(D[4] * sc, D[5] * sc), pk(D[6] * sc, D[7] * sc)};
    __builtin_amdgcn_raw_buffer_store_b128(nosent4(m), rWS, tb + (pair >> 1) * 1024 + (2 * (pair & 1) + h) * 256, 0, 16);
    u2 e = {nosent(pk(D[8] * sc, D[9] * sc)), nosent(pk(D[10] * sc, D[11] * sc))};
    if (l < 32) __builtin_amdgcn_raw_buffer_store_b64(e, rWS, tb + (8 + (pair >> 3)) * 1024 + ((pair >> 1) & 3) * 256 + (pair & 1) * 8, 0, 16);
  };
  u4 qm;
  u2 qe;
  float gate;
  if (__builtin_expect(w >= 4, 1)) {
    f4 qc[5][2];
    {
    LANES(tid);
    const u4 rX = mkrs(px, 2048 * 1280), rW1 = mkrs(isB ? pwb : pwa, 640 * 3840);
    const int xv0 = (32 * aw + (l >> 3)) * 1280 + (((l & 7) ^ (l >> 4)) << 4), xv1 = xv0 ^ 64;
    const int xs0 = tt * (128 * 1280);
    auto issx = [&](int s) {
#pragma unroll
      for (int i = 0; i < 4; ++i) dma(rX, ((9 - s) & 3) * SLOT + aw * 4096 + i * 1024, (i & 1) ? xv1 : xv0, xs0 + SK(s) * 128 + i * 10240);
    };
    issx(0);
    int wv[5];
    {
      const int base0 = isB ? 1280 + 160 * (2 * cq + cs - 2) : 1280 + 1280 * cs + 320 * cq;
      const int delta = isB ? 1120 : 0;
#pragma unroll
      for (int i = 0; i < 5; ++i) {
        int ci = 64 * i + l, rw = ci / 20, pos = ci - rw * 20;
        int t = pos ^ ((rw >> 2) & 2);
        int c = t >= 10 ? t ^ 1 : t;
        wv[i] = (16 * aw + rw) * 3840 + base0 + 16 * c + (c >= 10 ? delta : 0);
      }
    }
    auto issw = [&](int s) {
#pragma unroll
      for (int i = 0; i < 5; ++i) dma(rW1, ((9 - s) & 3) * SLOT + WOFF + aw * 5120 + i * 1024, wv[i], SK(s) * 245760);
    };
    auto iss2 = [&](int j) { dmas<5>(rWSd, W2L - 20480 * j + aw * 5120, l16, WS_W2 + pair * 102400 + j * 20480 + aw * 5120); };
    issw(0);
    STAMP(1);
#pragma unroll
    for (int s = 1; s < PRE; ++s) { issx(s); issw(s); }
    STAMP(2);
    const int xa0 = opq((32 * aw + r) * 128 + ((g ^ (r >> 1)) << 4)), xa1 = opq(xa0 ^ 64);
    const int xb0 = opq(xa0 + 2 * SLOT), xb1 = opq(xa1 + 2 * SLOT), ga = opq(GATE + g * 16);
    u4 xk[20][2];
    f4 gl[2] = {z4, z4};
    waitv<9 * (PRE - 1)>();
    bar();
    STAMP(3);
#pragma unroll
    for (int s = 0; s < 10; ++s) {
      const int xo = ((9 - s) & 1) * SLOT, x0 = ((9 - s) & 2) ? xb0 : xa0, x1 = ((9 - s) & 2) ? xb1 : xa1;
      xk[2 * s][0] = lr128(x0 + xo); xk[2 * s][1] = lr128(x0 + xo + 2048);
      xk[2 * s + 1][0] = lr128(x1 + xo); xk[2 * s + 1][1] = lr128(x1 + xo + 2048);
      u4 g0 = lr128(ga + s * 128), g1 = lr128(ga + s * 128 + 64);
      SB();
#pragma unroll
      for (int t = LASTI(s - 1) + 1; t <= LASTI(s); ++t) {
        issx(t);
        issw(t);
      }
      if (s == 7) { iss2(0); iss2(1); }
      if (s == 8) { iss2(2); iss2(3); }
      if (s == 9) iss2(4);
      SB();
      gl[0] = mf(g0, xk[2 * s][0], gl[0]); gl[1] = mf(g0, xk[2 * s][1], gl[1]);
      gl[0] = mf(g1, xk[2 * s + 1][0], gl[0]); gl[1] = mf(g1, xk[2 * s + 1][1], gl[1]);
      SB();
      waitl();
      {
        if (s < 9) waitvn(9 * (LASTI(s) - s - 1) + (s >= 7 ? 10 * (s - 6) : 0));
      }
      bar();
      STAMP(4 + s);
    }
#pragma unroll
    for (int i = 0; i < 5; ++i) qc[i][0] = qc[i][1] = z4;
    {
      float lg = (g & 1) ? gl[1][0] : gl[0][0];
      gate = __builtin_amdgcn_rcpf(1.f + ex2(-1.44269504089f * lg));
      asm volatile("" : "+v"(gate));
      const int rb = W2L - 40960 + (8 * g + 2 * q) * 80 + 32 + 40 * (p & 1);
      int a0 = opq(W2L - 40960 + (8 * g + 2 * q) * 80 + 8 * p);
      int r0 = opq(rb + ((p & 2) ? 5120 + 80 : 0)), r1 = opq(rb + ((p & 2) ? 5120 : 80));
      u4 f[5];
      auto rd2 = [&](int ks, int t) {
        int o = (ks < 12 ? 40960 : 81920) - 20480 * (ks >> 2) + (ks & 2) * 5120 + (ks & 1) * 2560;
        if (ks == 12 && t == 0) { a0 = opq(a0 - 40960); r0 = opq(r0 - 40960); r1 = opq(r1 - 40960); }
        if (t < 2) f[t] = cat(ltr(a0 + o + 40 * t), ltr(a0 + o + 40 * t + 80));
        else if (t < 4) f[t] = cat(ltr(a0 + o + 40 * (t - 2) + 5120 + 80), ltr(a0 + o + 40 * (t - 2) + 5120));
        else f[t] = cat(ltr(r0 + o), ltr(r1 + o));
      };
      waitv<20>();
      lset(PW + 4 * aw, 1);
      lwait4(PW, 1);
      STAMP(14);
#pragma unroll
      for (int t = 0; t < 5; ++t) rd2(0, t);
      SB();
#pragma unroll
      for (int ks = 0; ks < 20; ++ks) {
        const int j = ks >> 2;
        if ((ks & 3) == 1) { if (j < 4) waitvn(5 * (3 - j)); lset(PW + 4 * aw, j + 2); }
        if ((ks & 3) == 3 && j < 4) lwait4(PW, j + 2);
#pragma unroll
        for (int t = 0; t < 5; ++t) {
          qc[t][0] = mf(f[t], xk[ks][0], qc[t][0]);
          qc[t][1] = mf(f[t], xk[ks][1], qc[t][1]);
          SB();
          if (ks < 19) rd2(ks + 1, t);
          SB();
        }
        if (ks == 18) lset(PW + 4 * aw, 7);
      }
    }
    }
    STAMP(15);
    LANES(opq(tid));
    const float C = 0.22360679775f * 1.44269504089f;
    u4 xy;
    {
      f4 e0 = h ? phi4(qc[4][0]) : qc[4][0] * C, e1 = h ? phi4(qc[4][1]) : qc[4][1] * C;
      xy = widen(pk4(e0), pk4(e1));
      lw128(TQ + aw * 1792 + l16, widen(pk4(qc[1][0] * C), pk4(qc[1][1] * C)));
      lw64(TQ + aw * 1792 + 1024 + l * 8, u2{h ? 0x3F80u : xy.z, h ? 0u : xy.w});
      lw32(TQ + aw * 1792 + 1536 + l * 4, asu(gate));
      lset(QR + 4 * aw, 1);
      qm = widen(pk4(qc[0][0] * C), pk4(qc[0][1] * C));
      qe = u2{h ? 0x3F80u : xy.x, h ? 0u : xy.y};
    }
    {
      u4 p0 = widen(pk4(phi4(qc[2][0])), pk4(phi4(qc[2][1]))), p1 = widen(pk4(phi4(qc[3][0])), pk4(phi4(qc[3][1])));
      u4 o[16][1];
      gather<16, 1>(o, rWS, l16, [&](int i, int) { return WS_KVP + pair * 65536 + aw * 16384 + i * 1024; });
      STAMP(16);
      f4 s = __builtin_bit_cast(f4, o[0][0]);
#pragma unroll
      for (int i = 1; i < 16; ++i) s += __builtin_bit_cast(f4, o[i][0]);
      int e2 = (q & 1) * 1008 + (q < 2 ? 20 : 16 + p) * 48;
      int ka = aw < 2 ? aw * 1008 + r * 48 + g * 8 : (aw == 2 ? e2 + g * 8 : (g < 2 ? g * 1008 + r * 48 + 32 : e2 + 32));
      if (aw < 3 || g < 2 || (q & 1) == g - 2) lw64(KF + ka, pk4(s));
      linc(KFR);
      lwait(KFR, 4);
      const int kb = KF + (l & 31) * 48;
      f16v d0 = mf32(lr128(kb + 16 * h), p0, z16), d1 = mf32(lr128(kb + 1008 + 16 * h), p1, z16);
      d0 = mf8(lr64(kb + 32), u2{h ? xy.x : 0u, h ? xy.y : 0u}, d0);
      d1 = mf8(lr64(kb + 1008 + 32), u2{h ? xy.z : 0u, h ? xy.w : 0u}, d1);
      finish(d0, 1.f - gate, 1e-6f, 0, WS_OB, l);
      finish(d1, 1.f - gate, 1e-6f, 1, WS_OB, l);
    }
    STAMP(17);
  } else {
    LANES(tid);
    {
      unsigned o = (unsigned)((((2 * idx + w) & 7) * 64 + l) * 128);
      if (w < 2 && o < sz)
        asm volatile("s_mov_b32 m0, %2\n\ts_nop 0\n\tglobal_load_lds_dword %0, %1" ::"v"(o), "s"(pc), "s"(DUMP) : "memory");
    }
    u64 tag;
    asm volatile("s_load_dwordx2 %0, %1, 0x0" : "=s"(tag) : "s"(ws) : "memory");
    const int cm = w & 1, cn = w >> 1;
    {
      const bool isW = w < 2;
      const void* pw = w ? pwb : pwa;
      const void* po = u < 16 ? pwob : pwoa;
      const rsrc_t rS = mkr(isW ? pw : po, isW ? 640 * 3840 : 640 * 1280);
      const int pr5 = l / 5, pr10 = l / 10;
      const int v1 = ((pr5 >> 1) + 4 * ((pr5 ^ w) & 1)) * 3840 + 16 * (l - 5 * pr5), v2 = pr10 * 1280 + 16 * (l - 10 * pr10);
      const int oor = l < 40 ? 0 : 0x40000000;
      const int vs = (isW ? v1 : v2) | oor;
      const int jk = u < 16 ? 0 : 5, ro = u < 16 ? 0 : (u < 24 ? 20 : 30), hc = (u >> 3) == 2 ? 20 : 0, hk = u < 16 ? 20 : 0;
      u4 d[5];
      int dst[5];
#pragma unroll
      for (int i = 0; i < 5; ++i) {
        int kb = 40 * tt + 8 * i;
        int P = 10 * u + 5 * (w & 1) + i, rho = P >> 3, kh = rho & 1;
        int j = jk * kh + ((rho - ro) >> 1);
        int r1 = 80 * j + 10 * (P & 4) + 4 * (P & 3), r2 = 320 * j - 2544 + 40 * (P & 4) + 80 * (P & 1) + 20 * (P & 2);
        int row = (j < 8 ? r1 : r2) + hc + hk * kh;
        int s1 = kb * 3840 + 80 * pair, s2 = row * 1280 + 160 * cb;
        int d1 = WS_W2 + pair * 102400 + (kb >> 6) * 10240 + w * 5120 + (kb & 63) * 80, d2 = WS_WO + cb * 204800 + P * 640;
        dst[i] = isW ? d1 : d2;
        d[i] = __builtin_amdgcn_raw_buffer_load_b128(rS, vs, isW ? s1 : s2, 0);
      }
      if (w == 3) {
        const u4 rG = mkrs(pgw, 1280);
        dma(rG, GATE, l16, 0);
        asm volatile("s_mov_b32 m0, %0\n\ts_mov_b64 exec, 0xffff\n\tbuffer_load_dwordx4 %1, %2, %3 offen lds\n\ts_mov_b64 exec, -1" ::"s"(GATE + 1024), "v"(l16), "s"(rG), "s"(1024) : "memory");
      }
      if (w == 0 && l < 32) lw32(WD + 4 * l, 0);
#pragma unroll
      for (int i = 0; i < 6; ++i)
        __builtin_amdgcn_raw_buffer_store_b128(u4{~0u, ~0u, ~0u, ~0u}, rWS, WS_OB + bid * 24576 + w * 6144 + i * 1024 + l16, 0, 16);
#pragma unroll
      for (int i = 0; i < 5; ++i) __builtin_amdgcn_raw_buffer_store_b128(d[i], rWS, dst[i] + (l16 | oor), 0, 16);
    }
    int wa[2][5];
#pragma unroll
    for (int nt = 0; nt < 5; ++nt) {
      int byteA = 160 * cn + 32 * nt + 8 * p;
      int byteB = nt < 4 ? 160 * (nt >> 1) + 80 * cn + 40 * (nt & 1) + 8 * p : 160 * (p >> 1) + 80 * cn + 40 * (p & 1) + 32;
      int by = isB ? byteB : byteA;
      if (by >= 160) by ^= 16;
      by ^= (g & 1) << 5;
      wa[0][nt] = opq(WOFF + (8 * g + q) * 320 + by);
      wa[1][nt] = opq(wa[0][nt] + 2 * SLOT);
    }
    int xa[2][2];
    xa[0][0] = opq((64 * cm + r) * 128 + ((g ^ (r >> 1)) << 4)); xa[0][1] = opq(xa[0][0] ^ 64);
    xa[1][0] = opq(xa[0][0] + 2 * SLOT); xa[1][1] = opq(xa[0][1] + 2 * SLOT);
    u4 xf[2][4];
    u2 wf[2][5][2];
    f4 acc[4][5];
#pragma unroll
    for (int i = 0; i < 20; ++i) acc[i / 5][i % 5] = z4;
    auto rd = [&](int s, int kk, int i) {
      const int so = ((9 - s) & 1) * SLOT, hi = ((9 - s) >> 1) & 1;
      if (i < 4) xf[kk][i] = lr128(xa[hi][kk] + so + i * 2048);
      else wf[kk][(i - 4) >> 1][i & 1] = ltr(wa[hi][(i - 4) >> 1] + so + kk * 10240 + (i & 1) * 1280);
    };
    auto half = [&](int kk, int rs, int rkk) {
#pragma unroll
      for (int i = 0; i < 20; ++i) {
        acc[i & 3][i >> 2] = mf(xf[kk][i & 3], cat(wf[kk][i >> 2][0], wf[kk][i >> 2][1]), acc[i & 3][i >> 2]);
        if (i < 14 && rs < 10) rd(rs, rkk, i);
        SB();
      }
    };
    u4 fa0, fa1;
    STAMP(1);
    asm volatile("s_waitcnt vmcnt(11) lgkmcnt(0)" : "+s"(tag)::"memory");
    tag += 1;
    STAMP(2);
    bar();
    STAMP(3);
#pragma unroll
    for (int i = 0; i < 14; ++i) rd(0, 0, i);
    SB();
#pragma unroll
    for (int s = 0; s < 10; ++s) {
      half(0, s, 1);
      waitl();
      if (s == 2) waitv<0>();
      if (s == 6 && w == 0) {
        const unsigned tl = (unsigned)tag, th = (unsigned)(tag >> 32);
        bool bad = fa0.x != tl || fa0.y != th || fa0.z != tl || fa0.w != th || fa1.x != tl || fa1.y != th || fa1.z != tl || fa1.w != th;
        if (__builtin_amdgcn_ballot_w64(bad) != 0) {
#pragma unroll 1
          for (int i = 0; i < 4; ++i) poll(ws, WS_FA + (i * 64 + l) * 8, true, tag);
        }
        if (bid == 0 && l == 0) flag_store(ws, 0, tag);
      }
      bar(); SB();
      STAMP(4 + s);
      if (s == 1) CSTAMP(28);
      if (s == 5) CSTAMP(29);
      if (s == 2 && tid == 0) flag_store(ws, WS_FA + bid * 8, tag);
      if (s == 4 && w == 0) {
        fa0 = __builtin_amdgcn_raw_buffer_load_b128(rWS, l16, WS_FA, 16);
        fa1 = __builtin_amdgcn_raw_buffer_load_b128(rWS, l16, WS_FA + 1024, 16);
      }
      SB();
      half(1, s + 1, 0);
    }
    STAMP(14);
    if (!isB) {
      const bool isV = cs == 1;
      const int kvb = (isV ? WS_V : WS_K) + (4 * cq + 2 * cn) * (64 * BP) + (4 * tt + 2 * cm) * BP;
#pragma unroll
      for (int nt = 0; nt < 5; ++nt) {
        int col = 16 * nt + r, hl = col / 20, d = col - hl * 20;
        int ch = isV ? g ^ ((d >> 2) & 3) : g;
        int off = kvb + (hl >> 1) * (64 * BP) + (hl & 1) * HP + d * 64 + ch * 16;
        __builtin_amdgcn_raw_buffer_store_b128(cat(pk4(acc[0][nt]), pk4(acc[1][nt])), rWS, off, 0, 16);
        __builtin_amdgcn_raw_buffer_store_b128(cat(pk4(acc[2][nt]), pk4(acc[3][nt])), rWS, off + BP, 0, 16);
      }
      unsigned cv = isV ? 0x3F803F80u : 0u;
      if (l < 32) __builtin_amdgcn_raw_buffer_store_b128(u4{cv, cv, cv, cv}, rWS, kvb + g * (64 * BP) + q * HP + 1280 + p * 16, 0, 16);
      STAMP(15);
      waitv<0>();
      if (l == 0) {
        unsigned c = __hip_atomic_fetch_add((AS3 unsigned*)(size_t)CD, 1u, __ATOMIC_RELAXED, __HIP_MEMORY_SCOPE_WORKGROUP);
        if (c == 3) flag_store(ws, WS_FL + ((cq * 2 + cs) * 16 + tt) * 8, tag);
      }
    } else {
      f4 n0 = z4, n1 = z4, m2 = z4, m3 = z4, m4 = z4, m5 = z4, m6 = z4;
#pragma unroll
      for (int ks = 0; ks < 2; ++ks) {
        u4 k0 = cat(pk4(phi4(acc[2 * ks][0])), pk4(phi4(acc[2 * ks + 1][0])));
        u4 k1 = cat(pk4(phi4(acc[2 * ks][1])), pk4(phi4(acc[2 * ks + 1][1])));
        u4 kr = cat(pk4(phi4(acc[2 * ks][4])), pk4(phi4(acc[2 * ks + 1][4])));
        u4 v0 = cat(pk4(acc[2 * ks][2]), pk4(acc[2 * ks + 1][2]));
        u4 v1 = cat(pk4(acc[2 * ks][3]), pk4(acc[2 * ks + 1][3]));
        u4 vr = cat(pk4(acc[2 * ks][4]), pk4(acc[2 * ks + 1][4]));
        const unsigned one2 = 0x3F803F80u;
        if (r < 8) vr = u4{one2, one2, one2, one2};
        u4 kh;
#pragma unroll
        for (int i = 0; i < 4; ++i) kh[i] = __builtin_amdgcn_update_dpp(0u, kr[i], 0x118, 0xf, 0xf, true);
        n0 = mf(k0, v0, n0); n1 = mf(k1, v1, n1); m2 = mf(k0, vr, m2); m3 = mf(k1, vr, m3);
        m4 = mf(kr, v0, m4); m5 = mf(kr, v1, m5); m6 = mf(kh, vr, m6);
      }
      SB(); STAMP(25); SB();
      f4 n2 = (q & 1) ? m3 : m2;
      f4 n3 = g == 0 ? m4 : (g == 1 ? m5 : m6);
      lw128(w * 2048 + l16, __builtin_bit_cast(u4, cm ? n0 : n2));
      lw128(w * 2048 + 1024 + l16, __builtin_bit_cast(u4, cm ? n1 : n3));
      lset(RB + 4 * w, 1);
      STAMP(26);
      lwait(RB + 4 * (w ^ 1), 1);
      STAMP(27);
      f4 s0 = (cm ? n2 : n0) + __builtin_bit_cast(f4, lr128((w ^ 1) * 2048 + l16));
      f4 s1 = (cm ? n3 : n1) + __builtin_bit_cast(f4, lr128((w ^ 1) * 2048 + 1024 + l16));
      int ko = WS_KVP + (2 * (2 * cq + cs - 2) + cn) * 65536 + cm * 32768 + tt * 1024 + l16;
      __builtin_amdgcn_raw_buffer_store_b128(nosent4(__builtin_bit_cast(u4, s0)), rWS, ko, 0, 16);
      __builtin_amdgcn_raw_buffer_store_b128(nosent4(__builtin_bit_cast(u4, s1)), rWS, ko + 16384, 0, 16);
      linc(CD);
      STAMP(15);
      if (w == 0) lwait(CD, 4);
    }
    STAMP(16);
    if (w < 2) {
      const int lb = w ? VL : 0, nb = tt < 2 ? 8 - 4 * tt : 0;
      if (w) lwait4(PW, 4);
      if (nb) {
#pragma unroll 1
        for (int o = 0; o < nb * BP; o += 512) lw64(lb + o + l * 8, u2{0u, 0u});
        if (w == 0 && l < nb * 8) lw128((l >> 2) * HP + 1280 + (l & 3) * 16, u4{0xF149F149u, 0xF149F149u, 0xF149F149u, 0xF149F149u});
      }
      int t2 = tt - 2 + l;
      poll(ws, WS_FL + ((cq * 2 + w) * 16 + (t2 & 15)) * 8, l < 3 && t2 >= 0, tag);
      STAMP(17);
      const int src = (w ? WS_V : WS_K) + pair * (64 * BP) + (4 * tt - 8) * BP;
#pragma unroll 1
      for (int o = nb * BP; o < 12 * BP; o += 1024) dmas<1>(rWSd, lb + o, l16, src + o);
      waitv<0>();
      lset(w ? VR : KR, 1);
    } else {
      const int src = WS_WO + cb * 204800 + (w & 1) * 5120, lb = WOL + (w & 1) * 5120;
      dmas<5>(rWSd, lb + 4 * 10240, l16, src);
      lwait4(PW, 4);
      dmas<5>(rWSd, lb + 11 * 10240, l16, src + 10240);
      lwait4(PW, 5);
      dmas<5>(rWSd, lb + 9 * 10240, l16, src + 2 * 10240); dmas<5>(rWSd, lb + 10 * 10240, l16, src + 3 * 10240);
      lwait4(PW, 6);
      dmas<5>(rWSd, lb + 7 * 10240, l16, src + 4 * 10240); dmas<5>(rWSd, lb + 8 * 10240, l16, src + 5 * 10240);
      lwait4(PW, 7);
      dmas<5>(rWSd, lb + 5 * 10240, l16, src + 6 * 10240); dmas<5>(rWSd, lb + 6 * 10240, l16, src + 7 * 10240);
    }
    STAMP(18);
    lwait(QR + 4 * aw, 1);
    qm = lr128(TQ + aw * 1792 + l16);
    qe = lr64(TQ + aw * 1792 + 1024 + l * 8);
    gate = asf(lr32(TQ + aw * 1792 + 1536 + l * 4));
    __builtin_amdgcn_s_setprio(1);
    asm volatile(".Lfk_e2:" ::: "memory");
  }
  const int hh = w < 4;
  LANES(opq(tid));
  {
    unsigned mk[8];
#pragma unroll
    for (int i = 0; i < 8; ++i) {
      int dd = (l & 31) - 4 * h - (8 * (i >> 1) + 2 * (i & 1));
      mk[i] = dd > 0 ? 0xFFFFFFFFu : (dd == 0 ? 0xFFFFu : 0u);
    }
    const int kb = aw * BP + hh * HP + p * 16 + (g & 1) * 8;
    const int ka = kb + (8 * h + q) * 64, ke = kb + (h ? 20 : 16 + q) * 64;
    lwait(KR, 1);
    STAMP(19); CSTAMP(30);
    u4 km[9];
    u2 kr[9];
#pragma unroll
    for (int t = 0; t < 9; ++t) {
      km[t] = cat(ltr(ka + t * BP), ltr(ka + t * BP + 256));
      kr[t] = ltr(ke + t * BP);
    }
    linc(KD);
    f16v sd;
    {
      f16v s0 = mf32(km[0], qm, z16), s8 = mf32(km[8], qm, z16);
      s0 = mf8(kr[0], qe, s0); s8 = mf8(kr[8], qe, s8);
#pragma unroll
      for (int i = 0; i < 16; ++i) sd[i] = (8 * (i >> 2) + (i & 3) + 4 * h <= (l & 31)) ? s8[i] : s0[i];
    }
    float mx = sd[0];
#pragma unroll
    for (int i = 1; i < 16; ++i) mx = fmaxf(mx, sd[i]);
#pragma unroll
    for (int t = 1; t < 8; ++t) {
      f16v s = mf8(kr[t], qe, mf32(km[t], qm, z16));
#pragma unroll
      for (int i = 0; i < 16; ++i) mx = fmaxf(mx, s[i]);
    }
    {
      u2 t32 = swap32(asu(mx), asu(mx));
      mx = fmaxf(asf(t32.x), asf(t32.y));
    }
    STAMP(20);
    if ((w & 6) == 2) { waitv<0>(); linc(WA); }
    f16v nm;
#pragma unroll
    for (int i = 0; i < 16; ++i) nm[i] = -mx;
    const int d = l & 31;
    const int va0 = opq(VL + aw * BP + hh * HP + d * 64 + ((h ^ ((d >> 2) & 3)) << 4)), va1 = opq(va0 ^ 32);
    lwait(VR, 1);
    f16v O = z16;
    auto pv = [&](int t, const unsigned (&P)[8]) {
      O = mf32(lr128(va0 + t * BP), u4{P[0], P[1], P[4], P[5]}, O);
      O = mf32(lr128(va1 + t * BP), u4{P[2], P[3], P[6], P[7]}, O);
    };
    {
      unsigned P[8], P8[8];
#pragma unroll
      for (int i = 0; i < 8; ++i) {
        unsigned e = pk(ex2(sd[2 * i] - mx), ex2(sd[2 * i + 1] - mx));
        P8[i] = e & mk[i]; P[i] = e ^ P8[i];
      }
      pv(0, P); pv(8, P8);
    }
#pragma unroll
    for (int t = 1; t < 8; ++t) {
      f16v s = mf8(kr[t], qe, mf32(km[t], qm, nm));
      unsigned P[8];
#pragma unroll
      for (int i = 0; i < 8; ++i) P[i] = pk(ex2(s[2 * i]), ex2(s[2 * i + 1]));
      pv(t, P);
    }
    linc(VD);
    finish(O, gate, 0.f, hh, WS_OA, l);
  }
  __builtin_amdgcn_s_setprio(0);
  STAMP(21); CSTAMP(31);
  {
    const int m2 = w & 1, kh = (w >> 1) & 1;
    int pa[3];
    pa[0] = opq(WOL + kh * 5120 + (16 * (g >> 1) + 4 * (g & 1) + q) * 160 + 8 * p);
    pa[1] = opq(pa[0] + 6 * 10240); pa[2] = opq(pa[0] + 12 * 10240);
    const int ob = (4 * u + 2 * m2) * 20480, wsrc = WS_WO + cb * 204800;
    const int pb = WOL + (aw < 2 ? 5 + aw : aw - 2) * 10240 + l16;
    f4 oc[2][5];
#pragma unroll
    for (int i = 0; i < 5; ++i) oc[0][i] = oc[1][i] = z4;
    u4 wf[5][5];
    auto rdw = [&](int i, int m) {
#pragma unroll
      for (int nt = 0; nt < 5; ++nt) wf[i][nt] = cat(ltr(pa[m / 6] + m % 6 * 10240 + 32 * nt), ltr(pa[m / 6] + m % 6 * 10240 + 32 * nt + 1280));
    };
    auto mm = [&](int i, u4 o0, u4 o1) {
#pragma unroll
      for (int nt = 0; nt < 5; ++nt) { oc[0][nt] = mf(wf[i][nt], o0, oc[0][nt]); oc[1][nt] = mf(wf[i][nt], o1, oc[1][nt]); }
    };
    if (w < 4) {
      lwait(WA, 2);
      rdw(0, 4); rdw(1, 11); rdw(2, 9); rdw(3, 10); rdw(4, 7);
      linc(CP);
      lwait(KD, 8);
      dmas<10>(rWSd, WOL + w * 10240, l16, wsrc + (8 + w) * 10240);
      lwait(CP, 4);
      dmas<10>(rWSd, WOL + (w == 0 ? 4 : (w == 1 ? 11 : 7 + w)) * 10240, l16, wsrc + (12 + w) * 10240);
      { int st = (w == 3 ? 7 : 3 * w) * 1024; dmas<3>(rWSd, WOL + 7 * 10240 + st, l16, wsrc + 16 * 10240 + st); }
      STAMP(22);
      u4 o[10][2];
      gather<10, 2>(o, rWS, l16, [&](int i, int j) { return WS_OB + ob + j * 20480 + (10 * kh + i) * 1024; });
      waitv<0>();
      linc(WL);
      STAMP(23);
      mm(0, o[0][0], o[0][1]); SB(); rdw(0, 8);
      mm(1, o[1][0], o[1][1]); SB(); rdw(1, 5);
      mm(2, o[2][0], o[2][1]); SB(); rdw(2, 6);
      mm(3, o[3][0], o[3][1]); SB();
      lwait(WL, 4);
      rdw(3, 0);
      mm(4, o[4][0], o[4][1]); SB(); rdw(4, 1);
      linc(CE);
#pragma unroll
      for (int i = 0; i < 5; ++i) mm(i, o[5 + i][0], o[5 + i][1]);
      lwait(CE, 4);
#pragma unroll
      for (int i = 0; i < 10; ++i) lw128(pb + i * 1024, __builtin_bit_cast(u4, oc[i / 5][i % 5]));
      lset(PB + 4 * aw, 1);
      STAMP(24);
    } else {
      lwait(VD, 8);
      { int st = (aw == 3 ? 22 : 8 * aw) * 1024; dmas<8>(rWSd, VL + st, l16, wsrc + 17 * 10240 + st); }
      STAMP(22);
      lwait(WL, 4);
      rdw(0, 2); rdw(1, 3); rdw(2, 4); rdw(3, 11); rdw(4, 9);
      u4 o[5][2];
      gather<5, 2>(o, rWS, l16, [&](int i, int j) { return WS_OA + ob + j * 20480 + (10 + 5 * kh + i) * 1024; });
      waitv<0>();
      linc(VLD);
      STAMP(23);
      mm(0, o[0][0], o[0][1]); SB(); rdw(0, 10);
      mm(1, o[1][0], o[1][1]); SB(); rdw(1, 7);
      mm(2, o[2][0], o[2][1]); SB();
      lwait(VLD, 4);
      rdw(2, 12);
      mm(3, o[3][0], o[3][1]); SB(); rdw(3, 13);
      mm(4, o[4][0], o[4][1]); SB(); rdw(4, 14);
      STAMP(24);
      gather<5, 2>(o, rWS, l16, [&](int i, int j) { return WS_OA + ob + j * 20480 + (5 * kh + i) * 1024; });
      STAMP(25);
#pragma unroll
      for (int i = 0; i < 5; ++i) mm(i, o[i][0], o[i][1]);
      lwait(PB + 4 * aw, 1);
#pragma unroll
      for (int i = 0; i < 10; ++i) oc[i / 5][i % 5] += __builtin_bit_cast(f4, lr128(pb + i * 1024));
      STAMP(26);
      waitl();
#pragma unroll
      for (int i = 0; i < 5; ++i) lw128(pb + i * 1024, __builtin_bit_cast(u4, kh ? oc[0][i] : oc[1][i]));
      lset(EX + 4 * aw, 1);
      lwait(EX + 4 * (aw ^ 2), 1);
      const int px2 = WOL + (aw < 2 ? aw : 3 + aw) * 10240 + l16;
      u2 y[5];
#pragma unroll
      for (int i = 0; i < 5; ++i) y[i] = pk4((kh ? oc[1][i] : oc[0][i]) + __builtin_bit_cast(f4, lr128(px2 + i * 1024)));
      char* op = out + (size_t)((64 * u + 32 * m2 + 16 * kh + r) * 1280 + 160 * cb);
      *(u4*)(op + (g & 1) * 32 + h * 16) = widen(y[0], y[1]);
      *(u4*)(op + 64 + (g & 1) * 32 + h * 16) = widen(y[2], y[3]);
      *(u2*)(op + 128 + g * 8) = y[4];
      STAMP(27);
    }
  }
  asm volatile(".Lfk_e1:" ::: "memory");
}

extern "C" void kernel_launch(void* const* d_in, const int* in_sizes, int n_in, void* d_out, int out_size, void* d_ws,
                              size_t ws_size, hipStream_t stream) {
  (void)hipFuncSetAttribute((const void*)fused_kernel, hipFuncAttributeMaxDynamicSharedMemorySize, LDS_BYTES);
  fused_kernel<<<256, 512, LDS_BYTES, stream>>>(d_in[0], d_in[1], d_in[2], d_in[4], d_in[5], d_in[6], (char*)d_out, (char*)d_ws);
}
```

```cpp
#define CWA 0
#define HE 3
#define IMAJ
#define WHE 25
#define PRL 2
#define TS 1
#define TF 2
#define HJ 1
#include <hip/hip_runtime.h>
#include <stdint.h>
typedef __bf16 bf8 __attribute__((ext_vector_type(8)));
typedef __bf16 bf2 __attribute__((ext_vector_type(2)));
typedef float f4 __attribute__((ext_vector_type(4)));
typedef float f2 __attribute__((ext_vector_type(2)));
typedef float f16v __attribute__((ext_vector_type(16)));
typedef unsigned u4 __attribute__((ext_vector_type(4)));
typedef unsigned u2 __attribute__((ext_vector_type(2)));
typedef short s4 __attribute__((ext_vector_type(4)));
typedef unsigned long long u64;
#define AS3 __attribute__((address_space(3)))
typedef __amdgpu_buffer_rsrc_t rsrc_t;

constexpr int SLOT = 36864, WOFF = 16384, W2L = 137216, VL = 126976, BP = 2688, HP = 1344, RE = 32768, KF = 39936, EXC = 36864;
constexpr int TAB = 157696, CPL = 43008, CSUM = 104448, PKL = 147456, GATE = 160256, WD = 161536, DUMP = 161792, LDS_BYTES = 163840;
constexpr int PW = WD, KR = WD + 16, VR = WD + 20, KFR = WD + 24, AD = WD + 28, QR = WD + 32, RB = WD + 48, RF = WD + 64, PC = WD + 80,
              PH = WD + 144, RD = WD + 208, HD = WD + 224;
#define FS 16
#define FL(x) ((x) * FS)
constexpr int WS_FW = 4096, WS_FO = 8192, WS_FK = 12288, WS_FP = 16384, WS_FQ = 32768, WS_DBG = 18 << 20;
constexpr int WS_W2 = 1 << 20, WS_WO = 3 << 20, WS_K = 5 << 20, WS_V = 8 << 20;
constexpr int WS_OB = 11 << 20, WS_OA = WS_OB + 2621440, WS_KVP = WS_OA + 2621440, WS_BYTES = 18 << 20;
constexpr int OOB = 0x40000000;
#define SPIN 100000
constexpr int SPIN_MAX = SPIN;
#define GRP 4
#define NW1 0
#define TB 1
#define GS 5
#define PRC 1
#define PR1 1
#define PRH 0
#define NWR 0
#define WR0 76
constexpr int P1 = 11, N2 = 2 + 6 * P1, N3 = N2 + 3 * (19 - P1), N4 = 80 + 2 * P1;
__attribute__((always_inline)) constexpr int KD(int n) {
  if (n < 2) return 2 + n;
  if (n < N2) { int m = (n - 2) % 6; return m == 1 ? 2 : (m == 3 ? 3 : (m == 2 || m == 5 ? 1 : 0)); }
  if (n < N3) return 1 + (n - N2) % 3;
  return n < N4 ? 1 : 0;
}
__attribute__((always_inline)) constexpr int KI(int n) {
  if (n < 2) return 0;
  if (n < N2) { int p = (n - 2) / 6, m = (n - 2) % 6; return KD(n) >= 2 ? p + 1 : 2 * p + (m >= 4); }
  if (n < N3) return (KD(n) == 1 ? 2 * P1 : P1 + 1) + (n - N2) / 3;
  return n < N4 ? n + 40 - N4 : n - 80;
}
__attribute__((always_inline)) constexpr int MI(int n) { return n < N2 ? 2 * KI(n) + KD(n) : KI(n) + (n < N4 ? 2 * P1 : 40); }
__attribute__((always_inline)) constexpr int MT(int c) { return c < 4 * P1 ? c & 1 : c < 40 + 2 * P1; }
__attribute__((always_inline)) constexpr int MK(int c) { return c < 4 * P1 ? c >> 1 : (c < 40 + 2 * P1 ? c - 2 * P1 : c - 40); }
__attribute__((always_inline)) constexpr int GD(int s) { return s < 3 ? 0 : (s < 7 ? 16 : 4 * s + 4); }
#define RING 5
#define CKS 15
#define SWP 60
#define GN 38
#define L0 4
#define L1 31
#define L2 57
#define WRS 1
__attribute__((always_inline)) constexpr int WR(int n) {
  int v = n <= WR0 ? 0 : (n - WR0 + WRS - 1) / WRS;
  return v < NWR ? v : NWR;
}
template <int V> struct IC { static constexpr int value = V; };
template <int N, int E, class F> __device__ __forceinline__ void sfor(F&& f) {
  if constexpr (N < E) { f(IC<N>{}); sfor<N + 1, E>(f); }
}
#define TL 7
#define AUXW 16
#define AUXO 16
#define WH1 7
#define WH0 0
#define FSP 3
#define NOEARLY
#define WHI 3
#define WCI 4
#define WCE 25
__attribute__((always_inline)) constexpr int WH(int t) { return t <= 1 ? WH1 : (WH1 + WHI * (t - 1) < WHE && t < TL ? WH1 + WHI * (t - 1) : WHE); }
__attribute__((always_inline)) constexpr int WC(int t) { return t <= 1 ? NW1 : (NW1 + WCI * (t - 1) < WCE && t < TL ? NW1 + WCI * (t - 1) : WCE); }
#define SK(s) ((s) ^ ((s) < 8 ? grp : (grp & 1)))
#define PRT 1
#define CE
#define OX 0
#define CB 10
#define CS 5
#define PD 2
#define XC 20
#define XA 30
#define XB 40
#define XD 50
#define LS1
#define WSP 2
#define WS2 1
#define AC 2
#define AA 2
#define AB 4
#define KQX 2
#define J8 2
#define TG -1
#define J7 1
#define SF 3
#define HY 10
#define PKA 0
#define PF1 20
#define PF2 20
#define INI 5
#define PKS 1
__attribute__((always_inline)) constexpr int LS(int s) { return s + 2 + (s >= HY) < 9 ? s + 2 + (s >= HY) : 9; }
__attribute__((always_inline)) constexpr int WP(int s) { return s < 7 ? 0 : (s == 7 ? J7 : J8); }
__attribute__((always_inline)) constexpr int AFTER(int s) {
  int n = PF1 == -1, need = 0;
  for (int i = 0; i <= s; ++i) {
    if (i == SF) ++n;
    if (i == PF1) ++n;
    if (i == PF2) ++n;
    if (i == PKS && !PKA) { n += 4; if (s == SF - 1 && need < n) need = n; }
    for (int k = LS(i - 1) + 1; k <= LS(i); ++k) { n += 9; if (k == s + 1 && need < n) need = n; }
    if (i == PKS && PKA) { n += 4; if (s == SF - 1 && need < n) need = n; }
    n += 5 * (WP(i) - WP(i - 1));
  }
  return n - need;
}

template <int N> __device__ __forceinline__ void waitv() { asm volatile("s_waitcnt vmcnt(%0)" ::"i"(N) : "memory"); }
__device__ __forceinline__ void waitvn(int n) { asm volatile("s_waitcnt vmcnt(%0)" ::"i"(n) : "memory"); }
__device__ __forceinline__ void fence() { asm volatile("" ::: "memory"); }
__device__ __forceinline__ void waitl() { fence(); __builtin_amdgcn_s_waitcnt(0xC07F); fence(); }
__device__ __forceinline__ void bar() { fence(); __builtin_amdgcn_s_barrier(); fence(); }
__device__ __forceinline__ void dma(u4 rs, int lds, int voff, int soff) {
  asm volatile("s_mov_b32 m0, %0\n\ts_nop 0\n\tbuffer_load_dwordx4 %1, %2, %3 offen lds" ::"s"(lds), "v"(voff), "s"(rs), "s"(soff) : "memory");
}
template <int N> __device__ __forceinline__ void dmas(u4 rs, int lds, int voff, int soff) {
  asm volatile("s_mov_b32 m0, %0\n\ts_nop 0\n\tbuffer_load_dwordx4 %1, %2, %3 offen sc1 lds" ::"s"(lds), "v"(voff), "s"(rs), "s"(soff) : "memory");
  if constexpr (N > 1) asm volatile("buffer_load_dwordx4 %0, %1, %2 offen offset:1024 sc1 lds" ::"v"(voff), "s"(rs), "s"(soff) : "memory");
  if constexpr (N > 2) asm volatile("buffer_load_dwordx4 %0, %1, %2 offen offset:2048 sc1 lds" ::"v"(voff), "s"(rs), "s"(soff) : "memory");
  if constexpr (N > 3) asm volatile("buffer_load_dwordx4 %0, %1, %2 offen offset:3072 sc1 lds" ::"v"(voff), "s"(rs), "s"(soff) : "memory");
  if constexpr (N > 4) dmas<N - 4>(rs, lds + 4096, voff, soff + 4096);
}
__device__ __forceinline__ u4 mkrs(const void* p, unsigned bytes) {
  u64 a = (u64)p;
  return u4{(unsigned)a, (unsigned)(a >> 32) & 0xffffu, bytes, 0x00020000u};
}
__device__ __forceinline__ rsrc_t mkr(const void* p, int bytes) { return __builtin_amdgcn_make_buffer_rsrc((void*)p, 0, bytes, 0x00020000); }
#define AI __attribute__((always_inline))
#define SB() __builtin_amdgcn_sched_barrier(0)
__device__ __forceinline__ int opq(int x) { asm volatile("" : "+v"(x)); return x; }
#define LANES(t) const int l = (t) & 63, r = l & 15, g = l >> 4, q = r >> 2, p = r & 3, h = l >> 5
template <class T> __device__ __forceinline__ T sel(bool c, T a, T b) { return c ? a : b; }
__device__ __forceinline__ u4 lr128(int a) { return *(const AS3 u4*)(size_t)a; }
__device__ __forceinline__ u2 lr64(int a) { return *(const AS3 u2*)(size_t)a; }
__device__ __forceinline__ unsigned lr32(int a) { return *(const AS3 unsigned*)(size_t)a; }
__device__ __forceinline__ void lw128(int a, u4 v) { *(AS3 u4*)(size_t)a = v; }
__device__ __forceinline__ void lw64(int a, u2 v) { *(AS3 u2*)(size_t)a = v; }
__device__ __forceinline__ void lw32(int a, unsigned v) { *(AS3 unsigned*)(size_t)a = v; }
__device__ __forceinline__ u2 ltr(int a) {
  s4 v = __builtin_amdgcn_ds_read_tr16_b64_v4i16((AS3 s4*)(size_t)a);
  return __builtin_bit_cast(u2, v);
}
__device__ __forceinline__ void lset(int a, unsigned v) {
  asm volatile("s_mov_b64 exec, 1\n\tds_write_b32 %0, %1\n\ts_mov_b64 exec, -1" : "+v"(a), "+v"(v)::"memory");
}
__device__ __forceinline__ void linc(int a) {
  int v = 1;
  asm volatile("s_mov_b64 exec, 1\n\tds_add_u32 %0, %1\n\ts_mov_b64 exec, -1" : "+v"(a), "+v"(v)::"memory");
}
__device__ __forceinline__ void lwait(int a, unsigned v) {
  unsigned t = 0, c;
  asm volatile("s_mov_b32 %1, %4\n"
               "1:\n\tds_read_b32 %0, %2\n\ts_waitcnt lgkmcnt(0)\n\tv_cmp_gt_u32 vcc, %3, %0\n\ts_sub_u32 %1, %1, 1\n\ts_cbranch_vccz 2f\n\t"
               "s_cbranch_scc1 2f\n\ts_sleep 1\n\ts_branch 1b\n2:"
               : "+v"(t), "=&s"(c), "+v"(a) : "i"(v), "i"(SPIN_MAX) : "memory", "scc", "vcc");
}
__device__ __forceinline__ void lwait4(int a, unsigned v) { lwait(a + 4 * __builtin_amdgcn_mbcnt_lo(7, opq(0)), v); }
#define AM 0x3E
#define AR AM
#define PAT "v_mov_b64 %0, 0\n\tbuffer_atomic_or_x2 %0, %2, %4, 0 offen sc0"
#define PLD "buffer_load_dwordx2 %0, %2, %4, 0 offen sc1"
template <int N, int S> __device__ __forceinline__ u64 gload(u4 rs, int voff, u64 t) {
  if constexpr ((AM >> S) & 1)
    asm volatile("s_mov_b64 exec, %3\n\t" PAT "\n\ts_mov_b64 exec, -1" : "+v"(t) : "i"(0), "v"(voff), "i"((1 << N) - 1), "s"(rs) : "memory");
  else
    asm volatile("s_mov_b64 exec, %3\n\t" PLD "\n\ts_mov_b64 exec, -1" : "+v"(t) : "i"(0), "v"(voff), "i"((1 << N) - 1), "s"(rs) : "memory");
  return t;
}
#define PSL 1
#define PS0 12
#define PS1 PSL
#define PS2 1
#define PS4 1
#define PS5 PSL
#define PS8 1
#define GW(X) asm volatile("s_mov_b32 %1, %5\n\ts_branch 2f\n" \
               "1:\n\ts_sleep %6\n\ts_mov_b64 exec, vcc\n\t" X "\n\ts_mov_b64 exec, -1\n\ts_waitcnt vmcnt(0)\n" \
               "2:\n\tv_cmp_ne_u64 vcc, %3, %0\n\ts_sub_u32 %1, %1, 1\n\ts_cbranch_vccz 3f\n\ts_cbranch_scc0 1b\n3:" \
               : "+v"(t), "=&s"(c) : "v"(voff), "s"(tag), "s"(rs), "i"(SPIN_MAX), "i"(P) : "memory", "scc", "vcc")
template <int P, int S> __device__ __forceinline__ void gwait(u4 rs, int voff, u64 tag, u64 t) {
  unsigned c;
  if constexpr ((AR >> S) & 1) GW(PAT);
  else GW(PLD);
}
__device__ __forceinline__ void gflag(rsrc_t rs, int voff, u64 tag) {
  fence();
  __builtin_amdgcn_raw_buffer_store_b64(u2{(unsigned)tag, (unsigned)(tag >> 32)}, rs, voff, 0, 16);
  fence();
}
__device__ __forceinline__ u4 cat(u2 a, u2 b) { return u4{a.x, a.y, b.x, b.y}; }
__device__ __forceinline__ f4 mf(u4 a, u4 b, f4 c) {
  return __builtin_amdgcn_mfma_f32_16x16x32_bf16(__builtin_bit_cast(bf8, a), __builtin_bit_cast(bf8, b), c, 0, 0, 0);
}
__device__ __forceinline__ f16v mf32(u4 a, u4 b, f16v c) {
  return __builtin_amdgcn_mfma_f32_32x32x16_bf16(__builtin_bit_cast(bf8, a), __builtin_bit_cast(bf8, b), c, 0, 0, 0);
}
__device__ __forceinline__ f16v mf8(u2 a, u2 b, f16v c) {
  return __builtin_amdgcn_mfma_f32_32x32x8bf16_1k(__builtin_bit_cast(s4, a), __builtin_bit_cast(s4, b), c, 0, 0, 0);
}
__device__ __forceinline__ unsigned pk(float a, float b) {
  f2 v = {a, b};
  return __builtin_bit_cast(unsigned, __builtin_convertvector(v, bf2));
}
__device__ __forceinline__ u2 pk4(f4 v) { return u2{pk(v.x, v.y), pk(v.z, v.w)}; }
__device__ __forceinline__ float ex2(float x) { return __builtin_amdgcn_exp2f(x); }
__device__ __forceinline__ float mxf(float a, float b) { return __builtin_elementwise_maximum(a, b); }
__attribute__((always_inline)) constexpr u64 diag(int ix) { return (ix > 31 ? 0ull : (0xFFFFFFFFull << ix) & 0xFFFFFFFFull) | (ix + 4 > 31 ? 0ull : 0xFFFFFFFF00000000ull << (ix + 4)); }
__device__ __forceinline__ bool lm(u64 m) { return __builtin_amdgcn_inverse_ballot_w64(m); }
constexpr u64 VONE = 0x0FF000000FF00000ull;
constexpr unsigned EPS2 = 0x35863586u;
__device__ __forceinline__ float phi(float te) {
  return __builtin_amdgcn_fmed3f(ex2(__builtin_fmaf(te, 1.44269504089f, -1.44269504089f)), te, 1.f);
}
__device__ __forceinline__ f4 phi4(f4 v) { return f4{phi(v.x), phi(v.y), phi(v.z), phi(v.w)}; }
__device__ __forceinline__ u2 swap16(unsigned a, unsigned b) {
  auto r = __builtin_amdgcn_permlane16_swap(a, b, false, false);
  return u2{r[0], r[1]};
}
__device__ __forceinline__ u2 swap32(unsigned a, unsigned b) {
  auto r = __builtin_amdgcn_permlane32_swap(a, b, false, false);
  return u2{r[0], r[1]};
}
__device__ __forceinline__ u4 widen(u2 a, u2 b) {
  u2 t0 = swap16(a.x, b.x), t1 = swap16(a.y, b.y);
  return u4{t0.x, t1.x, t0.y, t1.y};
}
__device__ __forceinline__ float asf(unsigned x) { return __builtin_bit_cast(float, x); }
__device__ __forceinline__ unsigned asu(float x) { return __builtin_bit_cast(unsigned, x); }
#define STAMP(i)
#define CSTAMP(i)

__global__ __launch_bounds__(512, 2) __attribute__((target("no-packed-fp32-ops"))) void fused_kernel(
    const void* __restrict__ px, const void* __restrict__ pwa, const void* __restrict__ pwoa, const void* __restrict__ pwb,
    const void* __restrict__ pwob, const void* __restrict__ pgw, char* __restrict__ out, char* __restrict__ ws) {
  const int tid = threadIdx.x, w = __builtin_amdgcn_readfirstlane(tid >> 6), aw = w & 3;
  const int bid = blockIdx.x, xcd = bid & 7, idx = bid >> 3;
  const int cq = xcd >> 1, tt = 8 * (xcd & 1) + (idx & 7), cs = idx >> 3, pair = 4 * cq + cs;
  const int u = 4 * xcd + (idx >> 3), cb = idx & 7;
  const int grp = (tt + cs) & (GRP - 1);
  const bool isB = cs >= 2;
  const u4 rWSd = mkrs(ws, WS_BYTES);
  const rsrc_t rWS = mkr(ws, WS_BYTES);
  const f4 z4 = {0, 0, 0, 0};
  const f16v z16 = {0, 0, 0, 0, 0, 0, 0, 0, 0, 0, 0, 0, 0, 0, 0, 0};
  u64 pc; unsigned sz;
  asm volatile("s_getpc_b64 %0\n.Lfk_start:\n s_mov_b32 %1, .Lfk_e1-.Lfk_start\n s_max_u32 %1, %1, .Lfk_e2-.Lfk_start" : "=s"(pc), "=s"(sz)::"scc");
  STAMP(0);
  asm volatile("" ::"s"(px), "s"(pwa), "s"(pwoa), "s"(pwb), "s"(pwob), "s"(pgw), "s"(out), "s"(ws));
  u64 tag;
  const int obase = (4 * tt + aw) * 40960 + cq * 10240, fq = WS_FQ + (2 * tt + (aw >> 1)) * 512 + cq * 128 + (aw & 1) * 32 + cs * 8;
  int so, sr, ka, ke, ke0, va0, va1, fa, l16;
  unsigned mk[8];
  auto finish = [&](f16v D, float num, float eps, int base) AI {
    float den = asf(swap32(asu(D[8]), asu(D[8])).y);
    if (eps != 0.f) den += eps;
    float sc = num * __builtin_amdgcn_rcpf(den);
    u4 m = {pk(D[0] * sc, D[1] * sc), pk(D[2] * sc, D[3] * sc), pk(D[4] * sc, D[5] * sc), pk(D[6] * sc, D[7] * sc)};
    __builtin_amdgcn_raw_buffer_store_b128(m, rWS, so + base, 0, 16);
    return u2{pk(D[8] * sc, D[9] * sc), pk(D[10] * sc, D[11] * sc)};
  };
  auto finrem = [&](u4 e, int base) AI { __builtin_amdgcn_raw_buffer_store_b128(e, rWS, sr + base, 0, 16); };
  auto setup = [&](int i, int HH) AI {
    const int l = opq(tid) & 63, r = l & 15, g = l >> 4, q = r >> 2, p = r & 3, h = l >> 5, d = l & 31;
    const int b = aw * BP + HH * HP;
    if (i < 8) {
      const int ix = 8 * (i >> 1) + 2 * (i & 1);
      mk[i] = opq(sel(lm(diag(ix + 1)), 0xFFFFFFFFu, sel(lm(diag(ix)), 0xFFFFu, 0u)));
    }
    if (i == 8) ke = opq(b + (g & 1) * 8);
    if (i == 9) ka = opq(ke + p * 320 + (8 * h + q) * 16);
    if (i == 10) ke0 = opq(ke + sel(lm(0xFFFFFFFF00000000ull), 1280 + p * 16, p * 320 + 256 + q * 16));
    if (i == 11) ke = opq(ke0 + sel(lm(0x00F000F000000000ull), VL, 0));
    if (i == 12) { va0 = opq(VL + b + sel(lm(VONE), 1280, h * 320 + d * 16)); va1 = opq(va0 + sel(lm(VONE), 0, 640)); }
    if (i == 13) fa = opq(FL(WS_FQ + HH * 16384 + u * 512 + (aw ^ (HH * KQX)) * 128 + (l & 7) * 8));
    if (i == 14) so = opq(WS_OB + obase + ((cs & 2) + (g & 1)) * 1024 + (2 * (cs & 1) + h) * 256 + r * 16);
    if (i == 15) sr = opq(WS_OB + obase + 8192 + cs * 256 + (l & 16) * 64 + r * 16 + h * OOB);
  };
  u4 wo[25];
  const int wosrc = WS_WO + cb * 204800 + (w ^ ((w >> 2) * KQX)) * 25600;
  auto ct = [&](int n) AI { return n + cs >= 5 ? n + cs - 5 : n + cs; };
  auto ldwo = [&](int m) AI {
    wo[m] = __builtin_amdgcn_raw_buffer_load_b128(rWS, l16, wosrc + m / 5 * 5120 + ct(m % 5) * 1024, AUXW);
  };
  u4 qm;
  u2 qe;
  float gate;
  int kfh, kfl;
  u4 p0, p1, pe;
  u4 a0, a1;
  int kw, kv, qa, qb;
  auto setc = [&](int part) AI {
    LANES(opq(tid));
    if (part == 0) {
#pragma unroll
      for (int i = 0; i < 13; ++i) setup(i, 0);
    } else {
#pragma unroll
      for (int i = 13; i < 16; ++i) setup(i, 0);
      const unsigned one = (r & 1) ? 0x3F800000u : 0x3F80u;
#pragma unroll
      for (int i = 0; i < 4; ++i) { a0[i] = opq((r >> 1) == i ? one : 0u); a1[i] = opq((r >> 1) == i + 4 ? one : 0u); }
      const int e2 = (q & 1) * 1008 + sel(q < 2, 20, 16 + p) * 48;
      const int k3 = sel(g < 2, g * 1008 + r * 48 + 32, sel((q & 1) == g - 2, e2 + 32, 2560));
      kw = opq(KF + sel(aw < 2, aw * 1008 + r * 48 + g * 8, sel(aw == 2, e2 + g * 8, k3)));
      kv = opq(g * 512 + r * 16); qa = opq(CSUM + aw * 5120 + l16); qb = opq(CSUM + aw * 5120 + 1024 + l * 8);
    }
    SB();
  };
  auto tail = [&](auto role, u64 fl, int fp) AI {
    constexpr int HH = decltype(role)::value;
    {
      const u64 HI = 0xFFFFFFFF00000000ull;
      u4 km, kp[8];
      u2 kr;
      auto rdk = [&](int t) AI { km = cat(ltr(ka + t * BP), ltr(ka + t * BP + 64)); kr = ltr(ke + t * BP); };
      if (HH == 0) {
        lwait(QR + 4 * aw, 1);
        qm = lr128(qa);
        qe = lr64(qb);
        __builtin_amdgcn_s_setprio(PRC);
      }
      auto gather = [&]() AI {
        SB();
        waitv<0>();
        gwait<PS2, 2>(rWSd, fp, tag, fl);
#pragma unroll
        for (int i = 0; i < 8; ++i)
          kp[i] = __builtin_amdgcn_raw_buffer_load_b128(rWS, kv + (i & 1) * 256 + (i & 2) * 1024, WS_KVP + pair * 32768 + aw * 8192 + (i >> 2) * 4096, 16);
        SB();
        STAMP(27);
      };
      auto kvsum = [&]() AI {
        f4 s0 = z4, s1 = z4;
#pragma unroll
        for (int i = 0; i < 8; i += 2) { s0 = mf(a0, kp[i], s0); s1 = mf(a1, kp[i + 1], s1); }
        lw64(kw, pk4(s0 + s1));
        linc(KFR);
        STAMP(28);
        SB();
      };
      auto lin = [&]() AI {
        __builtin_amdgcn_s_setprio(PRL);
        STAMP(29);
        lwait(KFR, 4);
        STAMP(16);
        const u4 k0 = lr128(kfh), k1 = lr128(kfh + 1008), ep = {EPS2, EPS2, EPS2, EPS2};
        f16v d0 = mf32(k0, ep, z16), d1 = mf32(k1, ep, z16);
        d0 = mf32(k0, p0, d0);
        d1 = mf32(k1, p1, d1);
        d0 = mf8(lr64(kfl), u2{pe.x, pe.y}, d0);
        d1 = mf8(lr64(kfl + 1008), u2{pe.z, pe.w}, d1);
        u2 e0 = finish(d0, 1.f - gate, 1e-6f, 0);
        u2 e1 = finish(d1, 1.f - gate, 1e-6f, 4096);
        finrem(cat(e0, e1), 0);
        STAMP(17);
        __builtin_amdgcn_s_setprio(PRH);
        SB();
      };
      if (HH == 0 && TS < 0) kvsum();
      if (HH == 1 && TB < 0) lin();
      lwait(KR, 4);
      STAMP(19); if (HH == 0) CSTAMP(30);
      f16v S, O;
      u4 va, vb;
      {
        f16v s8 = mf32(cat(ltr(ka + 8 * BP), ltr(ka + 8 * BP + 64)), qm, z16);
        s8 = mf8(ltr(ke0 + 8 * BP), qe, s8);
        f16v s0 = mf32(cat(ltr(ka), ltr(ka + 64)), qm, z16);
        S = mf32(cat(ltr(ka + BP), ltr(ka + BP + 64)), qm, z16);
        if (HH == 0) { SB(); waitv<1>(); linc(VR); }
        if (HH == 0 && TG < 0) gather();
        lwait(VR, 4);
        STAMP(20);
        const unsigned nm = pk(0.f, -asf(swap32(asu(s8[0]), asu(s8[0])).x));
        qe.x |= sel(lm(HI), nm, 0u);
        s8 = mf8(ltr(ke + 8 * BP), u2{sel(lm(HI), nm, 0u), 0u}, s8);
        s0 = mf8(ltr(ke), qe, s0);
        S = mf8(ltr(ke + BP), qe, S);
        rdk(2);
        unsigned P[8], P8[8];
#pragma unroll
        for (int i = 0; i < 8; ++i) {
          const int ix = 8 * (i >> 1) + 2 * (i & 1);
          unsigned e = pk(ex2(sel(lm(diag(ix)), s8[2 * i], s0[2 * i])), ex2(sel(lm(diag(ix + 1)), s8[2 * i + 1], s0[2 * i + 1])));
          P8[i] = e & mk[i];
          P[i] = e ^ P8[i];
        }
        O = mf32(lr128(va0), u4{P[0], P[1], P[4], P[5]}, z16);
        O = mf32(lr128(va1), u4{P[2], P[3], P[6], P[7]}, O);
        O = mf32(lr128(va0 + 8 * BP), u4{P8[0], P8[1], P8[4], P8[5]}, O);
        O = mf32(lr128(va1 + 8 * BP), u4{P8[2], P8[3], P8[6], P8[7]}, O);
        va = lr128(va0 + BP); vb = lr128(va1 + BP);
      }
      SB();
      if (HH == 0 && TG == 0) gather();
      if (HH == 0 && TS == 0) kvsum();
      if (HH == 1 && TB == 0) lin();
#pragma unroll
      for (int t = 1; t < 8; ++t) {
        f16v N;
        u4 pa, pb;
        auto ld = [&](int k) AI {
#pragma unroll
          for (int i = (HH ? WH(t) : WC(t)) + k; i < (HH ? WH(t + 1) : WC(t + 1)); i += 4) ldwo(i);
          SB();
        };
        if (t < 7) N = mf32(km, qm, z16);
        SB();
        ld(0);
        pa.x = pk(ex2(S[0]), ex2(S[1])); pa.y = pk(ex2(S[2]), ex2(S[3]));
        SB();
        if (t < 7) N = mf8(kr, qe, N);
        SB();
        ld(3);
        if (t < 6) rdk(t + 2);
        pa.z = pk(ex2(S[8]), ex2(S[9])); pa.w = pk(ex2(S[10]), ex2(S[11]));
        pb.x = pk(ex2(S[4]), ex2(S[5]));
        SB();
        O = mf32(va, pa, O);
        SB();
        ld(1);
        if (t < 7) va = lr128(va0 + (t + 1) * BP);
        pb.y = pk(ex2(S[6]), ex2(S[7]));
        pb.z = pk(ex2(S[12]), ex2(S[13])); pb.w = pk(ex2(S[14]), ex2(S[15]));
        SB();
        O = mf32(vb, pb, O);
        SB();
        ld(2);
        if (t < 7) vb = lr128(va1 + (t + 1) * BP);
        if (HH == 0 && t == TS) kvsum();
        if (HH == 1 && t == TB) lin();
        if (HH == 1 && t == TF) { waitvn(WH(t + 1) - WH(TB + 1)); gflag(rWS, sel(lm(1), FL(fq), OOB), tag); STAMP(28); }
        if (HH == 1 && t == HE) linc(HD + 4);
        if (HH == 0 && t == 7) gate = asf(lr32(CSUM + aw * 5120 + 2048 + l16));
        S = N;
      }
      if (__builtin_expect((__builtin_amdgcn_ballot_w64(asu(O[8]) - 0x2B800000u >= 0x28000000u) & HI) != 0, 0)) {
        const int l = opq(tid) & 63, h = l >> 5;
        float mx = -3e38f;
        qe.x = h ? 0x3F80u : qe.x;
#pragma unroll 1
        for (int t = 0; t < 9; ++t) {
          const int o = t * BP, dq = (l & 31) - 4 * h, lo = t == 0 ? dq : -64, hi = t == 8 ? dq : 64;
          f16v s = mf8(ltr(ke + o), qe, mf32(cat(ltr(ka + o), ltr(ka + o + 64)), qm, z16));
#pragma unroll
          for (int i = 0; i < 16; ++i) {
            const int ix = 8 * (i >> 2) + (i & 3);
            mx = ix > lo && ix <= hi ? mxf(mx, s[i]) : mx;
          }
        }
        u2 t32 = swap32(asu(mx), asu(mx));
        mx = mxf(asf(t32.x), asf(t32.y));
        O = z16;
#pragma unroll 1
        for (int t = 0; t < 9; ++t) {
          const int o = t * BP, dq = (l & 31) - 4 * h, lo = t == 0 ? dq : -64, hi = t == 8 ? dq : 64;
          f16v s = mf8(ltr(ke + o), qe, mf32(cat(ltr(ka + o), ltr(ka + o + 64)), qm, z16));
#pragma unroll
          for (int i = 0; i < 16; ++i) {
            const int ix = 8 * (i >> 2) + (i & 3);
            s[i] = ix > lo && ix <= hi ? ex2(s[i] - mx) : 0.f;
          }
          O = mf32(lr128(va0 + o), u4{pk(s[0], s[1]), pk(s[2], s[3]), pk(s[8], s[9]), pk(s[10], s[11])}, O);
          O = mf32(lr128(va1 + o), u4{pk(s[4], s[5]), pk(s[6], s[7]), pk(s[12], s[13]), pk(s[14], s[15])}, O);
        }
      }
      if (HH == 1 && HE == 8) linc(HD + 4);
      fl = gload<8, 4 + HH>(rWSd, fa, tag);
      linc(AD);
      if (HH == 1) __builtin_amdgcn_s_setprio(2);
      u2 e = finish(O, gate, 0.f, WS_OA - WS_OB + HH * 4096);
      if (HH == 0) {
        lw64(RE + aw * 1024 + l16, e);
        lset(RF + 4 * aw, 1);
        __builtin_amdgcn_s_setprio(0);
      } else {
        lwait(RF + 4 * aw, 1);
        finrem(cat(lr64(RE + aw * 1024 + l16), e), WS_OA - WS_OB);
        linc(HD);
      }
      STAMP(21); if (HH == 0) CSTAMP(31);
    }
    LANES(opq(tid));
    const int fo = l == 0 ? FL(fq + 16384 + HH * 64) : OOB;
    const int ob = (HH ? WS_OA : WS_OB) + 2 * u * 40960 + (aw ^ (HH * KQX)) * 10240;
    u4 o[4][5];
    const int ow = aw ^ (OX);
    auto ldo = [&](int j, int i) AI {
      const int mt = ow ^ (3 - j);
      o[j][i] = __builtin_amdgcn_raw_buffer_load_b128(rWS, l16 + (i & 1) * 2048, ob + (mt >> 1) * 40960 + (mt & 1) * 1024 + (i >> 1) * 4096, AUXO);
    };
    u64 fl2;
    const int fb = opq(fa + FL(64));
    waitv<0>();
    gflag(rWS, fo, tag);
    if (HH == 1) __builtin_amdgcn_s_setprio(0);
    STAMP(22);
    f4 oc[4][5];
    constexpr int PX = HH ? PH : PC;
    auto part = [&](int k) AI { return HH ? (k & 2) * (VL / 2) + (k & 1) * 15360 : CPL + k * 15360; };
    int pa[3];
    if (HH == 1) {
#pragma unroll
      for (int m = WHE; m < 25; ++m) ldwo(m);
      SB();
    }
    if (HH == 0) {
      asm volatile("" : "+v"(fl)::"memory");
      if (__builtin_amdgcn_ballot_w64(fl != tag) != 0) lwait(HD + 4, 4);
    }
    gwait<HH ? PS5 : PS4, 4 + HH>(rWSd, fa, tag, fl);
    if (HH == 0) lwait(HD, CWA);
    STAMP(23);
    if (HH == 0) {
#pragma unroll
      for (int m = WCE; m < 25; ++m) ldwo(m);
#pragma unroll
      for (int i = 0; i < CB; ++i) { ldo(i / 5, i % 5); SB(); }
    } else {
#pragma unroll
      for (int i = 0; i < 8; ++i) ldo(i >> 1, i & 1);
      SB();
      fl2 = gload<8, 3>(rWSd, fb, tag);
    }
    SB();
#pragma unroll
    for (int j = 0; j < 3; ++j) pa[j] = opq(part(aw ^ (3 - j)) + j * 5120 + l16);
    const int pw = opq(part(aw) + l16), pc = opq(CSUM + aw * 5120 + l16), ph = opq(PX + 16 * aw + 4 * p);
    if (HH == 0) {
      lwait4(PW, 5);
    } else {
#pragma unroll
      for (int j = 0; j < 4; ++j) {
#pragma unroll
        for (int nt = 0; nt < 5; ++nt) oc[j][nt] = mf(wo[nt], o[j][0], z4);
#pragma unroll
        for (int nt = 0; nt < 5; ++nt) oc[j][nt] = mf(wo[5 + nt], o[j][1], oc[j][nt]);
        SB();
        if (j == HJ) {
          asm volatile("s_waitcnt vmcnt(0)" : "+v"(fl2)::"memory");
          gwait<PS8, 3>(rWSd, fb, tag, fl2);
#pragma unroll
          for (int i = 0; i < 12; ++i) ldo(i / 3, 2 + i % 3);
          SB();
        }
        SB();
      }
      lwait(AD, 8);
    }
    STAMP(24);
    unsigned pv;
    f4 e0[5];
    if (HH == 0) {
      sfor<0, 100>([&](auto slot) AI {
        constexpr int n = decltype(slot)::value, j = n / 25, nt = n % 5, i = n % 25 / 5, m = CB + n / CS;
        constexpr int t = (n - 20 - PD) / 25, c = n - 20 - PD - 25 * t;
        constexpr int x = n < 80 ? 0 : (n < 90 ? 1 : 2), k = n - (x == 0 ? 68 : 70 + 10 * x);
        auto rdp = [&](int q) AI { e0[q] = __builtin_bit_cast(f4, lr128(pa[x] + q * 1024)); };
        oc[j][nt] = mf(wo[5 * i + nt], o[j][i], i ? oc[j][nt] : (j == 3 ? e0[nt] : z4));
        SB();
        if constexpr (n % CS == 0 && m < 20) ldo(m / 5, m % 5);
        if constexpr (n >= 20 + PD && c < 5) lw128(t < 3 ? pw + t * 5120 + c * 1024 : pc + c * 1024, __builtin_bit_cast(u4, oc[t][c]));
        if constexpr (n >= 20 + PD && t < 3 && c == 4) lset(PX + 16 * (aw ^ (3 - t)) + 4 * t, 1);
        if (k == 0) { fence(); pv = lr32(ph); fence(); SB(); rdp(0); }
        if (k == 1) { rdp(1); rdp(2); }
        if (k == 2) { rdp(3); rdp(4); }
        if (k == 4) {
          if (__builtin_expect((__builtin_amdgcn_ballot_w64(pv == 0) & (1 << x)) != 0, 0)) {
            lwait(PX + 16 * aw + 4 * x, 1);
#pragma unroll
            for (int q = 0; q < 5; ++q) rdp(q);
          }
        }
        if constexpr (x > 0 && k >= 4 && k < 9) {
          oc[3][k - 4] += e0[k - 4];
          asm volatile("" : "+v"(oc[3][k - 4]));
        }
        SB();
      });
      STAMP(25);
#pragma unroll
      for (int nt = 5 - PD; nt < 5; ++nt) lw128(pc + nt * 1024, __builtin_bit_cast(u4, oc[3][nt]));
      lset(PH + 16 * aw + 12, 1);
      STAMP(26);
      gflag(rWS, tid + bid == 0 ? 0 : OOB, tag);
      STAMP(30);
    } else {
      const rsrc_t rO = mkr(out, 2048 * 1280);
      int op, os[3], fx[3], one;
      u2 y[5];
      f4 e[2][5];
      sfor<0, 60>([&](auto slot) AI {
        constexpr int n = decltype(slot)::value, j = n / 15, nt = n % 5, i = 2 + n % 15 / 5;
        constexpr int t = (n - 10 - PD) / 15, c = n - 10 - PD - 15 * t;
        oc[j][nt] = mf(wo[5 * i + nt], o[j][i], oc[j][nt]);
        SB();
        constexpr int ws = t < 2 ? WSP : WS2;
        if constexpr (n >= 10 + PD && t < 3 && c % ws == 0 && c / ws < 5) lw128(pw + t * 5120 + c / ws * 1024, __builtin_bit_cast(u4, oc[t][c / ws]));
        if constexpr (n >= 10 + PD && t < 3 && c == 4 * ws) { fence(); lw32(fx[t], one); fence(); }
        if constexpr (n >= 8 && n < 11) fx[n - 8] = opq(PX + 16 * (aw ^ (11 - n)) + 4 * (n - 8));
        if (n == 11) one = opq(1);
        sfor<0, 4>([&](auto part) AI {
          constexpr int x = decltype(part)::value, k = n - (x == 3 ? XC : (x == 0 ? XA : (x == 1 ? XB : XD)));
          constexpr int as = x == 3 ? AC : (x == 0 ? AA : (x == 1 ? AB : 4));
          const int src = x == 3 ? pc : pa[x % 3];
          auto rdp = [&](int q) AI { e[x & 1][q] = __builtin_bit_cast(f4, lr128(src + q * 1024)); };
          if (k == 0) { fence(); pv = lr32(ph); fence(); SB(); rdp(0); }
          if (k == 1) { rdp(1); rdp(2); }
          if (k == 2) { rdp(3); rdp(4); }
          if (k == 4) {
            if (__builtin_expect((__builtin_amdgcn_ballot_w64(pv == 0) & (1 << x)) != 0, 0)) {
              lwait(PX + 16 * aw + 4 * x, 1);
#pragma unroll
              for (int q = 0; q < 5; ++q) rdp(q);
            }
          }
          if constexpr (k >= 4 && as * (k - 4) < 20) {
#pragma unroll
            for (int q = as * (k - 4); q < as * (k - 3) && q < 20; ++q) oc[3][q >> 2][q & 3] += e[x & 1][q >> 2][q & 3];
#pragma unroll
            for (int q = as * (k - 4) >> 2; q <= (as * (k - 3) - 1 >> 2) && q < 5; ++q) asm volatile("" : "+v"(oc[3][q]));
          }
        });
        if (n == 1) op = opq((64 * u + 16 * ow + r) * 1280 + 160 * cb);
        if (n == 3) os[2] = opq(op + ct(4) * 32 + g * 8);
        if (n == 4) op = opq(op + h * 16);
        if (n == 5) os[0] = opq(op + sel(lm(0xFFFF0000FFFF0000ull), ct(1), ct(0)) * 32);
        if (n == 6) os[1] = opq(op + sel(lm(0xFFFF0000FFFF0000ull), ct(3), ct(2)) * 32);
        if (n == 59) { y[0] = pk4(oc[3][0]); y[1] = pk4(oc[3][1]); }
        SB();
      });
      STAMP(25);
      __builtin_amdgcn_raw_buffer_store_b128(widen(y[0], y[1]), rO, os[0], 0, 0);
      SB();
      __builtin_amdgcn_raw_buffer_store_b128(widen(pk4(oc[3][2]), pk4(oc[3][3])), rO, os[1], 0, 0);
      SB();
      __builtin_amdgcn_raw_buffer_store_b64(pk4(oc[3][4]), rO, os[2], 0, 0);
      STAMP(27);
    }
  };
  if (__builtin_expect(w >= 4, 1)) {
    f16v D0 = z16, D1;
    f4 E0, E1, G0 = z4, G1 = z4;
    const float C = 0.22360679775f * 1.44269504089f;
    const u64 HI = 0xFFFFFFFF00000000ull;
    __builtin_amdgcn_s_waitcnt(0x0F70);
    {
    LANES(tid);
    l16 = l * 16;
    const u4 rX = mkrs(px, 2048 * 1280), rW1 = mkrs(isB ? pwb : pwa, 640 * 3840);
    const int xv0 = (32 * aw + (l >> 3)) * 1280 + (((l & 7) ^ (l >> 4)) << 4), xv1 = xv0 ^ 64;
    const int xs0 = tt * (128 * 1280);
    auto issx = [&](int s) AI {
#pragma unroll
      for (int i = 0; i < 4; ++i) dma(rX, ((9 - s) & 3) * SLOT + aw * 4096 + i * 1024, (i & 1) ? xv1 : xv0, xs0 + SK(s) * 128 + i * 10240);
    };
    issx(0);
    int wv[5];
    {
      const int base0 = sel(isB, 1280 + 160 * (2 * cq + cs - 2), 1280 + 1280 * cs + 320 * cq);
      const int delta = isB ? 1120 : 0;
#pragma unroll
      for (int i = 0; i < 5; ++i) {
        int ci = 64 * i + l, rw = ci / 20, pos = ci - rw * 20;
        int t = pos ^ ((rw >> 2) & 2);
        int c = t >= 10 ? t ^ 1 : t;
        wv[i] = (16 * aw + rw) * 3840 + base0 + 16 * c + (c >= 10 ? delta : 0);
      }
    }
    auto isw1 = [&](int s, int i) AI { dma(rW1, ((9 - s) & 3) * SLOT + WOFF + aw * 5120 + i * 1024, wv[i], SK(s) * 245760); };
    auto issw = [&](int s) AI {
#pragma unroll
      for (int i = 0; i < 5; ++i) isw1(s, i);
    };
    auto iss1 = [&](int j, int i) AI {
      const int st = SK(2 * j + 1 - (aw >> 1));
      dmas<1>(rWSd, W2L - 20480 * j + aw * 5120 + i * 1024, l16, WS_W2 + pair * 102400 + st * 10240 + (aw & 1) * 5120 + i * 1024);
    };
    auto iss2 = [&](int j) AI {
      const int st = SK(2 * j + 1 - (aw >> 1));
      dmas<5>(rWSd, W2L - 20480 * j + aw * 5120, l16, WS_W2 + pair * 102400 + st * 10240 + (aw & 1) * 5120);
    };
    issw(0);
    u64 ep;
    asm volatile("s_load_dwordx2 %0, %1, 0x0" : "=s"(ep) : "s"(ws) : "memory");
    STAMP(1);
    {
      int pv1, pv2[2], pv3;
      const int l5 = l / 5, l10 = l / 10;
      pv1 = (40 * tt + 10 * aw + l5) * 3840 + 80 * pair + 16 * (l - 5 * l5);
#pragma unroll
      for (int i = 0; i < 2; ++i) {
        int rr = 10 * aw + 5 * i + l10, e = rr & 7, o = 5 * (u & 3) + (rr >> 3);
        int rm = 80 * ((o >> 2) & 1) + 40 * ((o >> 1) & 1) + 20 * (o >> 3) + 4 * (o & 1) + 8 * (e >> 2);
        int rx = 40 * (o & 3) + 20 * (e >> 2) + 16;
        pv2[i] = (40 * (u & 12) + (e & 3) + (o < 16 ? rm : rx)) * 1280 + 160 * cb + 16 * (l - 10 * l10);
      }
      int c = 20 * aw + l, sg = c >> 3;
      pv3 = (SK(sg) << 7) + (c & 7) * 16;
      const u4 rA = mkrs(pwa, 640 * 3840), rB = mkrs(pwb, 640 * 3840), rO = mkrs(u < 16 ? pwob : pwoa, 640 * 1280), rG = mkrs(pgw, 1280);
      asm volatile("s_mov_b32 m0, %0\n\ts_mov_b32 exec_hi, 0x3ffff\n\tbuffer_load_dwordx4 %1, %2, 0 offen lds\n\t"
                   "s_add_u32 m0, m0, 3200\n\ts_nop 0\n\tbuffer_load_dwordx4 %1, %3, 0 offen lds\n\t"
                   "s_mov_b32 m0, %4\n\ts_nop 0\n\tbuffer_load_dwordx4 %5, %7, 0 offen lds\n\t"
                   "s_add_u32 m0, m0, 800\n\ts_nop 0\n\tbuffer_load_dwordx4 %6, %7, 0 offen lds\n\t"
                   "s_mov_b32 m0, %8\n\ts_mov_b64 exec, 0xfffff\n\tbuffer_load_dwordx4 %9, %10, 0 offen lds\n\ts_mov_b64 exec, -1"
                   ::"s"(PKL + aw * 800), "v"(pv1), "s"(rA), "s"(rB), "s"(PKL + 6400 + aw * 1600), "v"(pv2[0]), "v"(pv2[1]), "s"(rO),
                     "s"(GATE + aw * 320), "v"(pv3), "s"(rG) : "memory", "scc");
    }
    issx(1); issw(1);
    u64 pfa;
    int pfs;
    {
      const int j = min(l - 32, 23), rr = j >= 12, jj = j - 12 * rr, seg = (jj * 11) >> 5, ln = jj - 3 * seg;
      const u64 b = sel(l < 32, (u64)px, sel(seg < 2, (u64)pwa, (u64)pwb));
      pfs = sel(l < 32, 128, 245760);
      pfa = b + (unsigned)(sel(l < 32, (128 * tt + 32 * cs + l) * 1280, (2 * idx + rr) * 3840 + 1280 + 1280 * (seg & 1) + ((320 * cq) & ~127) + 128 * ln) +
                           (4 + aw) * pfs);
      pfs *= 4 - (aw & 2);
    }
    auto pf = [&]() AI { asm volatile("s_mov_b32 m0, %1\n\ts_nop 0\n\tglobal_load_lds_dword %0, off" ::"v"(pfa), "s"(DUMP) : "memory"); };
    if (PF1 == -1) pf();
    STAMP(2);
    int pr[2][2], pd[2][2];
#pragma unroll
    for (int i = 0; i < 2; ++i) {
      int c = 8 * aw + 4 * i + g, oc = c / 5, cg = c - 5 * oc, bad = c < 25 ? r * 16 : OOB;
      int ko = 5 * tt + oc, o = 5 * u + oc;
      pr[0][i] = opq(PKL + (8 * oc + q) * 80 + sel(cg < 4, (cg >> 1) * 3200 + (cg & 1) * 40 + 16 * (p & 1) + 4 * (p & 2), (p & 1) * 3200 + 32 + 20 * (p & 2)));
      pd[0][i] = opq(WS_W2 + pair * 102400 + (ko >> 3) * 10240 + (3 - ((ko >> 1) & 3)) * 2560 + (cg >> 1) * 1024 + (cg & 1) * 256 +
                     (ko & 1) * (cg < 4 ? 512 : 256) + bad);
      pr[1][i] = opq(PKL + 6400 + (8 * oc + q) * 160 + 32 * cg + 8 * p);
      pd[1][i] = opq(WS_WO + cb * 204800 + (o >> 2) * 5120 + cg * 1024 + (o & 3) * 256 + bad);
    }
    int xa[2][2];
    const int gk = 2 * (g & 1) + (g >> 1);
    xa[0][0] = opq((32 * aw + r) * 128 + ((gk ^ (r >> 1)) << 4)); xa[0][1] = opq(xa[0][0] ^ 64);
    xa[1][0] = opq(xa[0][0] + 2 * SLOT); xa[1][1] = opq(xa[0][1] + 2 * SLOT);
    const int ga = opq(GATE + gk * 16);
    const int fs = opq(sel(tid == 256, FL(WS_FW + pair * 128 + tt * 8), sel(tid == 257, FL(WS_FO + cb * 256 + u * 8), OOB)));
    u4 xk[40];
    int a0, a2, pwa4;
    auto init = [&]() AI {
      const float e6 = h ? 1.f : 0.f;
#pragma unroll
      for (int i = 0; i < 16; ++i) D1[i] = 1.f;
      E0 = f4{e6, e6, e6, e6}; E1 = E0;
      asm volatile("" : "+v"(D1), "+v"(E0), "+v"(E1));
      a0 = opq(W2L - 30720 + l16);
      a2 = opq(W2L - 30720 + 2048 + 2560 - (g & 1) * 2560 + h * 256 + (r & 3) * 16 + (r & 4) * 32 + (r & 8) * 8);
      pwa4 = opq(PW + 4 * (l & 3));
      SB();
    };
    u4 gf[8];
    auto gate1 = [&](int i) AI {
      if (i & 1) G1 = mf(gf[(i >> 1) & 7], xk[i], G1);
      else G0 = mf(gf[(i >> 1) & 7], xk[i], G0);
      asm volatile("" : "+v"(G0), "+v"(G1));
      SB();
    };
    if (INI == -9) init();
    asm volatile("s_waitcnt vmcnt(%1) lgkmcnt(0)" : "+s"(ep) : "i"(14 + (PF1 == -1)) : "memory");
    bar();
    tag = ep + 1;
    STAMP(3);
#pragma unroll
    for (int s = 0; s < 10; ++s) {
      u4 pt[4];
#pragma unroll
      for (int k = 0; k < 4; ++k) xk[4 * s + k] = lr128(xa[((9 - s) >> 1) & 1][k >> 1] + ((9 - s) & 1) * SLOT + (k & 1) * 2048);
#pragma unroll
      for (int k = GD(s - 1) / 2; k < GD(s) / 2 && s < 9; ++k) gf[k & 7] = lr128(ga + k * 64);
      if (s == PKS) {
#pragma unroll
        for (int i = 0; i < 4; ++i) pt[i] = cat(ltr(pr[i & 1][i >> 1]), ltr(pr[i & 1][i >> 1] + ((i & 1) ? 640 : 320)));
      }
      SB();
      if (s == 9) break;
      if (s == SF) gflag(rWS, fs, tag);
      if (s == -INI) init();
      if (s == PF1) pf();
      if (s == PF2) { pfa += (unsigned)pfs; pf(); }
      auto pst = [&]() AI {
        SB();
#pragma unroll
        for (int i = 0; i < 4; ++i) __builtin_amdgcn_raw_buffer_store_b128(pt[i], rWS, pd[i & 1][i >> 1], 0, 16);
        SB();
      };
      if (s == PKS && !PKA) pst();
      fence();
#pragma unroll
      for (int k = LS(s - 1) + 1; k <= LS(s); ++k) {
        issx(k);
        if (s != 3) issw(k);
      }
      if (s == PKS && PKA) pst();
      if (s == 3 || s == 7) {
#pragma unroll
        for (int i = 0; i < 5; ++i) {
          SB();
#pragma unroll
          for (int k = GD(s - 1) + 3 * i; k < GD(s - 1) + 3 * i + 3; ++k) gate1(k);
          if (s == 3) isw1(5, i);
          else iss1(0, i);
        }
        SB();
        gate1(GD(s) - 1);
      }
#pragma unroll
      for (int j = WP(s - 1) + (s == 7); j < WP(s); ++j) iss2(j);
      if (s == INI) init();
      SB();
      if (s != 3 && s != 7) {
#pragma unroll
        for (int i = GD(s - 1); i < GD(s); ++i) gate1(i);
      }
      waitl();
      waitvn(AFTER(s));
      bar();
      STAMP(4 + s);
    }
#pragma unroll
      for (int j = J8; j < 5; ++j) {
        iss2(j);
        if (j == J8) { waitv<5 * J8>(); lset(PW + 4 * aw, 1); }
      }
      u4 f[RING], fe[2];
      auto rdm = [&](int c) AI {
        if (c == 40) a0 = opq(a0 - 51200);
        f[c % RING] = lr128(a0 + (19 - MK(c) % 20) * 2560 + MT(c) * 1024);
      };
      auto rde = [&](int k) AI {
        if (k == 10) a2 = opq(a2 - 51200);
        fe[k & 1] = lr128(a2 + (9 - k % 10) * 5120);
      };
      auto xsw = [&](int k, int i) AI {
        const u2 t = swap16(xk[2 * k][i], xk[2 * k + 1][i]);
        xk[2 * k][i] = t.x; xk[2 * k + 1][i] = t.y;
      };
      STAMP(13);
      lwait4(PW, 1);
      STAMP(14); CSTAMP(30);
      rde(0); rdm(0); rde(1);
#pragma unroll
      for (int n = 1; n < RING; ++n) rdm(n);
      SB();
      unsigned pv;
      u4 tb[3];
      u2 ts;
      int ta, tq0, tq1;
      float ph, fm, fb, y0, y1, x0, x1, D2[8];
      const int tq = CSUM + aw * 5120;
      sfor<0, 120>([&](auto slot) AI {
        constexpr int n = decltype(slot)::value, ks = KI(n), t = KD(n), j = n / 24;
        constexpr int ls = j == 0 ? L0 : (j == 1 ? L1 : L2), ck = 24 * j + (j ? CKS : 11);
        if (n == ls && j < 3) { waitvn((j == 0 ? 15 : (j == 1 ? 10 : 0)) + WR(n)); lset(PW + 4 * aw, j + 2); }
        if (n == ck - 3 && j < 3) pv = lr32(pwa4);
        if (n == ck && j < 3) {
          if (__builtin_expect(__builtin_amdgcn_ballot_w64(pv < (unsigned)(j + 2)) != 0, 0)) lwait4(PW, j + 2);
        }
        if (t == 0) D0 = mf32(f[MI(n) % RING], xk[ks], D0);
        if (t == 1) D1 = mf32(f[MI(n) % RING], xk[ks], D1);
        if (t == 2) E0 = mf(fe[ks & 1], xk[2 * ks], E0);
        if (t == 3) E1 = mf(fe[ks & 1], xk[2 * ks + 1], E1);
        SB();
        if constexpr (t < 2 && MI(n) + RING < 80) rdm(MI(n) + RING);
        if constexpr (t == 3 && ks + 2 < 20 && !((ks + 2) % 4 == 0 && ks < 12)) rde(ks + 2);
        if (n == ck && j < 3) rde(4 * j + 4);
        constexpr int sw = n == 1 ? 0 : (n < N2 ? ((n - 2) % 6 >= 4 ? ks / 2 + 1 : -1) : (n > N2 && n <= N3 && t == 1 ? ks - P1 : -1));
        if constexpr (sw >= 0) {
          if (n < SWP) { SB(); __builtin_amdgcn_s_setprio(2); SB(); }
          if (n == 1 || n > N2 || t == 0) { xsw(sw, 0); xsw(sw, 1); }
          if (n == 1 || n > N2 || t == 1) { xsw(sw, 2); xsw(sw, 3); }
          if (n < SWP) { SB(); __builtin_amdgcn_s_setprio(0); SB(); }
        }
        if (n == GN - 6) gf[2] = lr128(ga + 18 * 64);
        if (n == GN + 5) gf[3] = lr128(ga + 19 * 64);
        if constexpr (n >= GN && n < GN + 24 && (n - GN) % 6 == 0) { SB(); gate1(36 + (n - GN) / 6); }
#pragma unroll
        for (int i = WR(n); i < WR(n + 1); ++i) ldwo(i);
        if (t == 0 && n > 60) asm volatile("" ::"v"(D0));
        if (n == 20) ta = opq(TAB + l16);
        if (n == 21 || n == 22) tb[n - 21] = lr128(ta + (n - 21) * 1024);
        if (n == 23) tb[2] = lr128(ta + (DUMP + 1024 - TAB));
        if (n == 26) ts = lr64(TAB + 2048 + (l16 >> 1));
        if (n == 32) { ka = opq(tb[2].x + aw * BP + HP); ke = opq(tb[2].y + aw * BP + HP); }
        if (n == 34) { ke0 = opq(tb[2].z + aw * BP + HP); va0 = opq(tb[2].w + aw * BP + HP); }
        if (n == 38) { so = opq(ts.x + aw * 40960); sr = opq(ts.y + aw * 40960); }
        if (n == 40) va1 = opq(va0 + sel(lm(VONE), 0, 640));
        if (n == 44) { fa = opq((l16 << 3) & 0x380); fa = opq(fa + FL(WS_FQ + 16384 + u * 512 + (aw ^ KQX) * 128)); }
        if (n == 46) kfl = opq(KF + 32 + 3 * (l16 & 0x1F0));
        if (n == 50) kfh = opq(kfl + sel(lm(HI), -16, -32));
        if (n == 52) { tq1 = opq(tq + 1024 + (l16 >> 1)); tq0 = opq(tq + l16); }
        if (n == 52) {
#pragma unroll
          for (int i = 0; i < 8; ++i) mk[i] = tb[i >> 2][i & 3];
        }
        if (n == GN + 24) gate = asf(opq(asu(ex2(-1.44269504089f * sel(lm(0xFFFF0000FFFF0000ull), G1[0], G0[0])))));
        if (n == GN + 26) gate = asf(opq(asu(__builtin_amdgcn_rcpf(1.f + gate))));
        if (n == 90) lw32(tq0 + 2048, asu(gate));
        if (n == 115) lset(PW + 4 * aw, 5);
        if (n == 56) { fm = asf(opq(asu(sel(lm(HI), 1.44269504089f, C)))); fb = asf(opq(asu(sel(lm(HI), -1.44269504089f, 0.f)))); }
        if (n == N3 + 1) {
#pragma unroll
          for (int i = 0; i < 4; ++i) { const u2 e = swap16(asu(E0[i]), asu(E1[i])); D2[i] = asf(opq(e.x)); D2[4 + i] = asf(opq(e.y)); }
        }
        constexpr int m = n - N3 - 2, k = n - N4 - 2;
        if constexpr (m >= 0 && m < 10) {
#pragma unroll
          for (int o = 2 * m; o < 2 * m + 2; ++o) {
            const int i = o / 5 * 2;
            if (o % 5 == 0) { y0 = asf(opq(asu(__builtin_fmaf(D2[i], fm, fb)))); y1 = asf(opq(asu(__builtin_fmaf(D2[i + 1], fm, fb)))); }
            if (o % 5 == 1) { x0 = asf(opq(asu(ex2(y0)))); x1 = asf(opq(asu(ex2(y1)))); }
            if (o % 5 == 2) ts[(i >> 1) & 1] = opq(sel(lm(HI), i & 2 ? 0u : 0x3F80u, pk(y0, y1)));
            if (o % 5 == 3) { x0 = asf(opq(asu(__builtin_amdgcn_fmed3f(x0, D2[i], 1.f)))); x1 = asf(opq(asu(__builtin_amdgcn_fmed3f(x1, D2[i + 1], 1.f)))); }
            if (o % 5 == 4) pe[i >> 1] = opq(sel(lm(HI), pk(x0, x1), EPS2));
            if (o == 8) lw64(tq1, ts);
            if (o == 19) qe = ts;
          }
        }
        if (k == -1) asm volatile("" ::"v"(D1));
        if constexpr (k >= 0) {
          if (k & 1) (k < 8 ? p0 : p1)[(k >> 1) & 3] = opq(pk(ph, phi(D1[k])));
          else ph = asf(opq(asu(phi(D1[k]))));
        }
        SB();
      });
      STAMP(15); CSTAMP(31);
#pragma unroll
      for (int i = WR(120); i < WH0; ++i) ldwo(i);
      SB();
      lw128(tq0, u4{pk(D0[0] * C, D0[1] * C), pk(D0[2] * C, D0[3] * C), pk(D0[4] * C, D0[5] * C), pk(D0[6] * C, D0[7] * C)});
      lset(QR + 4 * aw, 1);
      SB();
#pragma unroll
      for (int i = WH0; i < WH1; ++i) ldwo(i);
#pragma unroll
      for (int i = 0; i < 4; ++i) qm[i] = opq(pk(D0[8 + 2 * i] * C, D0[9 + 2 * i] * C));
    }
    STAMP(18);
    tail(IC<1>{}, 0, 0);
  } else {
    LANES(tid);
    l16 = l * 16;
    {
      unsigned o = (unsigned)((((2 * idx + w) & 7) * 64 + l) * 128);
      if (w < 2 && o < sz)
        asm volatile("s_mov_b32 m0, %2\n\ts_nop 0\n\tglobal_load_lds_dword %0, %1" ::"v"(o), "s"(pc), "s"(DUMP) : "memory");
    }
    u64 ep;
    asm volatile("s_load_dwordx2 %0, %1, 0x0" : "=s"(ep) : "s"(ws) : "memory");
    const int cm = w & 1, cn = w >> 1;
    lw32(WD + 4 * l, 0);
    int wa[2][5];
#pragma unroll
    for (int nt = 0; nt < 5; ++nt) {
      int byteA = 160 * cn + 32 * nt + 8 * p;
      int byteB = nt < 4 ? 160 * (nt >> 1) + 80 * cn + 40 * (nt & 1) + 8 * p : 160 * (p >> 1) + 80 * cn + 40 * (p & 1) + 32;
      int by = isB ? byteB : byteA;
      if (by >= 160) by ^= 16;
      by ^= (g & 1) << 5;
      wa[0][nt] = opq(WOFF + (8 * g + q) * 320 + by);
      wa[1][nt] = opq(wa[0][nt] + 2 * SLOT);
    }
    int xa[2][2];
    xa[0][0] = opq((64 * cm + r) * 128 + ((g ^ (r >> 1)) << 4)); xa[0][1] = opq(xa[0][0] ^ 64);
    xa[1][0] = opq(xa[0][0] + 2 * SLOT); xa[1][1] = opq(xa[0][1] + 2 * SLOT);
    const int fw = opq(FL(WS_FW + pair * 128 + w * 32 + p * 8)), fw2 = opq(FL(WS_FO + cb * 256 + w * 64 + (l & 7) * 8));
    u64 fc;
    setc(0);
    setc(1);
    const bool isV = cs == 1;
    const int kvb = (isV ? WS_V : WS_K) + (4 * cq + 2 * cn) * (64 * BP) + (4 * tt + 2 * cm) * BP, pp = 2 * (2 * cq + cs - 2) + cn;
    int ko[6];
#pragma unroll
    for (int nt = 0; nt < 5; ++nt) {
      int col = 16 * nt + r, hl = col / 20, d = col - hl * 20;
      ko[nt] = opq(kvb + (hl >> 1) * (64 * BP) + (hl & 1) * HP + d * 16 + g * 320);
    }
    ko[5] = opq(sel(isB, WS_KVP + pp * 32768 + cm * 16384 + tt * 512 + h * 256 + r * 16 + (g & 1) * 8,
                    kvb + (g & 1) * (64 * BP) + q * HP + 1280 + p * 16 + h * OOB));
    const int b0 = 4 * tt - 8 + 3 * w;
    const int fo = opq(sel(l == 0, FL(sel(isB, WS_FP + pp * 256 + cm * 128 + tt * 8, WS_FK + cs * 2048 + cq * 512 + cn * 256 + tt * 16 + cm * 8)), OOB));
    const int fk = opq(FL(WS_FK + (l & 1) * 2048 + cq * 512 + (cs >> 1) * 256 + max((b0 + (l & 2)) >> 1, 0) * 8));
    const int fp = opq(FL(WS_FP + pair * 256 + (w >> 1) * 128 + r * 8));
    const int ba = opq(sel(l < 24 && b0 + (l >> 3) < 0, (6 * w + (l >> 2)) * HP + 1280 + (l & 3) * 16, DUMP + l16));
    const int ex = opq(EXC + w * 4096 + l16);
    u4 xf[2][4];
    u2 wf[2][5][2];
    f4 acc[4][5];
    {
      const float e0 = isB ? 1.f : 0.f, e1 = r < 8 ? e0 : 0.f;
#pragma unroll
      for (int i = 0; i < 20; ++i) { float v = i % 5 < 2 ? e0 : (i % 5 == 4 ? e1 : 0.f); acc[i / 5][i % 5] = f4{v, v, v, v}; }
    }
    auto rd = [&](int s, int kk, int i) AI {
      const int so = ((9 - s) & 1) * SLOT, hi = ((9 - s) >> 1) & 1;
      if (i < 4) xf[kk][i] = lr128(xa[hi][kk] + so + i * 2048);
      else wf[kk][(i - 4) >> 1][i & 1] = ltr(wa[hi][(i - 4) >> 1] + so + kk * 10240 + (i & 1) * 1280);
    };
    auto half = [&](int kk, int rs, int rkk) AI {
#pragma unroll
      for (int i = 0; i < 20; ++i) {
        acc[i & 3][i >> 2] = mf(xf[kk][i & 3], cat(wf[kk][i >> 2][0], wf[kk][i >> 2][1]), acc[i & 3][i >> 2]);
        if (i < 14 && rs < 10) rd(rs, rkk, i);
        SB();
      }
    };
    STAMP(1);
    asm volatile("s_waitcnt lgkmcnt(0)" : "+s"(ep)::"memory");
    tag = ep + 1;
    STAMP(2);
    bar();
    STAMP(3);
#pragma unroll
    for (int i = 0; i < 14; ++i) rd(0, 0, i);
    SB();
#pragma unroll
    for (int s = 0; s < 10; ++s) {
      half(0, s, 1);
      if (s == 6 || s == 8) { waitv<0>(); gwait<PS0, 0>(rWSd, s == 6 ? fw : fw2, tag, fc); }
      if (s < 9) { waitl(); bar(); } else linc(RD);
      if (s == GS) fc = gload<4, 0>(rWSd, fw, tag);
      if (s == 7) fc = gload<8, 0>(rWSd, fw2, tag);
      if (s == 8) __builtin_amdgcn_s_setprio(PR1);
      if (s == 2 && w == 0) {
        lw128(TAB + l16, u4{mk[0], mk[1], mk[2], mk[3]});
        lw128(TAB + 1024 + l16, u4{mk[4], mk[5], mk[6], mk[7]});
        lw128(DUMP + 1024 + l16, u4{(unsigned)ka, (unsigned)ke, (unsigned)ke0, (unsigned)va0});
        lw64(TAB + 2048 + (l16 >> 1), u2{(unsigned)so, (unsigned)sr});
      }
      SB();
      STAMP(4 + s);
      if (s == 1) CSTAMP(28);
      if (s == 5) CSTAMP(29);
      SB();
      half(1, s + 1, 0);
    }
    STAMP(14);
    bool early = false;
    if (!isB) {
#pragma unroll
      for (int nt = 0; nt < 5; ++nt) {
        __builtin_amdgcn_raw_buffer_store_b128(cat(pk4(acc[0][nt]), pk4(acc[1][nt])), rWS, ko[nt], 0, 16);
        __builtin_amdgcn_raw_buffer_store_b128(cat(pk4(acc[2][nt]), pk4(acc[3][nt])), rWS, ko[nt] + BP, 0, 16);
      }
      unsigned cv = isV ? 0x3F803F80u : 0u;
      __builtin_amdgcn_raw_buffer_store_b128(u4{cv, cv, cv, cv}, rWS, ko[5], 0, 16);
    } else {
      u4 v0[2], v1[2], vr[2], k0[2], k1[2], kr[2];
#pragma unroll
      for (int ks = 0; ks < 2; ++ks) {
        v0[ks] = cat(pk4(acc[2 * ks][2]), pk4(acc[2 * ks + 1][2]));
        v1[ks] = cat(pk4(acc[2 * ks][3]), pk4(acc[2 * ks + 1][3]));
        vr[ks] = cat(pk4(acc[2 * ks][4]), pk4(acc[2 * ks + 1][4]));
#pragma unroll
        for (int i = 0; i < 4; ++i) vr[ks][i] = sel(lm(0x00FF00FF00FF00FFull), 0x3F803F80u, vr[ks][i]);
      }
#pragma unroll
      for (int i = 0; i < 8; ++i)
        acc[i >> 2][4][i & 3] = asf(__builtin_amdgcn_update_dpp(asu(acc[i >> 2][4][i & 3]), asu(acc[2 + (i >> 2)][4][i & 3]), 0x118, 0xf, 0xf, false));
      kr[0] = cat(pk4(phi4(acc[0][4])), pk4(phi4(acc[1][4])));
#pragma unroll
      for (int i = 0; i < 4; ++i) kr[1][i] = __builtin_amdgcn_update_dpp(0u, kr[0][i], 0x128, 0xf, 0xf, true);
#pragma unroll
      for (int ks = 0; ks < 2; ++ks) {
        if (ks == FSP) { SB(); fc = gload<4, 1>(rWSd, fk, tag); SB(); }
        k0[ks] = cat(pk4(phi4(acc[2 * ks][0])), pk4(phi4(acc[2 * ks + 1][0])));
        k1[ks] = cat(pk4(phi4(acc[2 * ks][1])), pk4(phi4(acc[2 * ks + 1][1])));
      }
      const u4 ep = {EPS2, EPS2, EPS2, EPS2};
      SB();
      if (FSP == 2) { fc = gload<4, 1>(rWSd, fk, tag); SB(); }
      if (FSP > 2) fc = ~tag;
      f4 n0 = mf(ep, v0[0], z4), n1 = mf(ep, v1[0], z4), m2 = mf(ep, vr[0], z4);
      n0 = mf(ep, v0[1], n0); n1 = mf(ep, v1[1], n1); m2 = mf(ep, vr[1], m2);
      f4 m4 = n0, m5 = n1, m3 = m2, m6 = m2;
#pragma unroll
      for (int ks = 0; ks < 2; ++ks) {
        m4 = mf(kr[ks], v0[ks], m4); m5 = mf(kr[ks], v1[ks], m5); m6 = mf(kr[ks ^ 1], vr[ks], m6); m3 = mf(k1[ks], vr[ks], m3);
        m2 = mf(k0[ks], vr[ks], m2); n0 = mf(k0[ks], v0[ks], n0); n1 = mf(k1[ks], v1[ks], n1);
      }
      SB();
      f4 n2, n3;
#pragma unroll
      for (int i = 0; i < 4; ++i) {
        n3[i] = sel(lm(0xFFFF), m4[i], sel(lm(0xFFFFFFFF), m5[i], m6[i]));
        n2[i] = sel(lm(0xF0F0F0F0F0F0F0F0ull), m3[i], m2[i]);
      }
      lw128(ex + 3072, __builtin_bit_cast(u4, n3));
      lw128(ex + 2048, __builtin_bit_cast(u4, n2));
      lw128(ex, __builtin_bit_cast(u4, n0));
      lw128(ex + 1024, __builtin_bit_cast(u4, n1));
      lset(RB + 4 * w, 1);
      lwait(RB + 4 * (w ^ 1), 1);
      const int er = ex - cm * 2048;
      f4 s0 = __builtin_bit_cast(f4, lr128(er)) + __builtin_bit_cast(f4, lr128(er + 4096));
      f4 s1 = __builtin_bit_cast(f4, lr128(er + 1024)) + __builtin_bit_cast(f4, lr128(er + 5120));
      asm volatile("s_waitcnt vmcnt(0)" : "+v"(fc)::"memory");
      __builtin_amdgcn_raw_buffer_store_b64(pk4(s0), rWS, ko[5], 0, 16);
      __builtin_amdgcn_raw_buffer_store_b64(pk4(s1), rWS, ko[5] + 8192, 0, 16);
    }
    STAMP(15);
    SB();
    if (!early) {
      fc = gload<4, 1>(rWSd, opq(isB ? fk : OOB), tag);
      waitv<1>();
      gflag(rWS, fo, tag);
      asm volatile("s_waitcnt vmcnt(1)" : "+v"(fc)::"memory");
      fc = sel(lm(sel(isB, ~0ull, ~15ull)), fc, ~tag);
    }
    STAMP(16);
    lwait4(PW, 3);
    lwait(RD, 4);
    {
      gwait<PS1, 1>(rWSd, fk, tag, fc);
      STAMP(17);
#pragma unroll
      for (int kv = 0; kv < 2; ++kv) {
        const u4 rK = mkrs(ws + (kv ? WS_V : WS_K) + pair * (64 * BP), 64 * BP);
        int vo = b0 * BP + l16;
        asm volatile("s_mov_b32 m0, %1\n\ts_nop 0\n\t"
                     ".rept 7\n\tbuffer_load_dwordx4 %0, %2, 0 offen sc1 lds\n\ts_add_u32 m0, m0, 1024\n\tv_add_u32 %0, 1024, %0\n\t.endr\n\t"
                     "s_mov_b32 exec_hi, 0xffffff\n\tbuffer_load_dwordx4 %0, %2, 0 offen sc1 lds\n\ts_mov_b32 exec_hi, -1"
                     : "+v"(vo) : "s"(kv * VL + 3 * w * BP), "s"(rK) : "memory", "scc");
        if (kv == 0) { waitv<8>(); gflag(rWS, early ? fo : OOB, tag); }
      }
      u64 fl = gload<16, 2>(rWSd, fp, tag);
      waitv<10>();
      lw128(ba, u4{0xF149F149u, 0xF149F149u, 0xF149F149u, 0xF149F149u});
      linc(KR);
      STAMP(18);
      tail(IC<0>{}, fl, fp);
    }
    asm volatile(".Lfk_e2:" ::: "memory");
  }
  asm volatile(".Lfk_e1:" ::: "memory");
}

extern "C" void kernel_launch(void* const* d_in, const int* in_sizes, int n_in, void* d_out, int out_size, void* d_ws,
                              size_t ws_size, hipStream_t stream) {
  (void)hipFuncSetAttribute((const void*)fused_kernel, hipFuncAttributeMaxDynamicSharedMemorySize, LDS_BYTES);
  fused_kernel<<<256, 512, LDS_BYTES, stream>>>(d_in[0], d_in[1], d_in[2], d_in[4], d_in[5], d_in[6], (char*)d_out, (char*)d_ws);
}
```
